```python
import torch

def _softmax(x, axis=-1):
    return torch.softmax(x, dim=axis)
def _gelu(x):
    return torch.nn.functional.gelu(x, approximate="tanh")
def _silu(x):
    return torch.nn.functional.silu(x)
def _sigmoid(x):
    return torch.sigmoid(x)
def _layernorm(x, axis=-1, eps=1e-5):
    m = x.mean(dim=axis, keepdim=True)
    v = x.var(dim=axis, keepdim=True, unbiased=False)
    return (x - m) / torch.sqrt(v + eps)
def _scatter_add(tgt, idx, src, axis=0):
    return tgt.clone().index_add_(axis, idx, src)
def _segment_sum(src, seg, n):
    out = torch.zeros((n,) + src.shape[1:], dtype=src.dtype)
    return out.index_add_(0, seg, src)
def _pad(x, pw):
    flat = tuple(v for pair in reversed(pw) for v in pair)
    return torch.nn.functional.pad(x, flat)

kernel_name = 'sequential_preLN__mha_block__dense_activation_relu__prompt'

def setup_inputs():

    _w = (lambda a: (lambda t: t.to(torch.bfloat16) if t.is_floating_point() else t)(torch.as_tensor(a)))
    def _A_setup():
        B, S, D, H = 1, 2048, 512, 32
        return {
            "x": _w((lambda *s: torch.randn(*s, dtype=torch.bfloat16) * 0.05)(B, S, D)),
            "wqkv": _w((lambda *s: torch.randn(*s, dtype=torch.bfloat16) * 0.05)(D, 3 * D)),
            "wo": _w((lambda *s: torch.randn(*s, dtype=torch.bfloat16) * 0.05)(D, D)),
        }
    _A_in = _A_setup()

    def _B_setup():
        B, S, D, Dff = 1, 2048, 512, 1792
        return {
            "x": _w((lambda *s: torch.randn(*s, dtype=torch.bfloat16) * 0.05)(B, S, D)),
            "w1": _w((lambda *s: torch.randn(*s, dtype=torch.bfloat16) * 0.05)(D, Dff)),
            "w2": _w((lambda *s: torch.randn(*s, dtype=torch.bfloat16) * 0.05)(Dff, D)),
        }
    _B_in = _B_setup()

    return {
        "A_x": _A_in["x"],
        "A_wqkv": _A_in["wqkv"],
        "A_wo": _A_in["wo"],
        "B_x": _B_in["x"],
        "B_w1": _B_in["w1"],
        "B_w2": _B_in["w2"],
    }

def reference(A_x, A_wqkv, A_wo, B_x, B_w1, B_w2):
    def _A_ref(x, wqkv, wo):
        B, S, D = x.shape
        H = 32
        Dh = D // H
        qkv = (lambda x, *d: x.permute(*d))((x @ wqkv).reshape(B, S, 3, H, Dh), 2, 0, 3, 1, 4)
        q, k, v = qkv[0], qkv[1], qkv[2]
        scores = torch.einsum("bhqd,bhkd->bhqk", q, k) * (Dh ** -0.5)
        p = _softmax(scores, axis=-1)
        o = torch.einsum("bhqk,bhkd->bhqd", p, v)
        return (lambda x, *d: x.permute(*d))(o, 0, 2, 1, 3).reshape(B, S, D) @ wo

    def _B_ref(x, w1, w2):
        h = torch.einsum("bsd,df->bsf", x, w1)
        zero = torch.zeros_like(h)
        h = torch.maximum(h, zero)
        return torch.einsum("bsf,fd->bsd", h, w2)

    x = A_x
    h = x + _A_ref(_layernorm(x, axis=-1), A_wqkv, A_wo)
    h2 = h + _B_ref(_layernorm(h, axis=-1), B_w1, B_w2)
    return h2
```

```cpp
#include <hip/hip_runtime.h>
#include <hip/hip_bf16.h>
typedef __hip_bfloat16 bf16;
constexpr int S=2048, D=512, H=32, DH=16, F=1792;
__device__ inline float b2f(bf16 x){return __bfloat162float(x);}
__global__ void ln_k(const bf16* x, bf16* y){
  int row=blockIdx.x; int t=threadIdx.x;
  float v[8]; float s=0;
  for(int i=0;i<8;i++){v[i]=b2f(x[row*D+t*8+i]); s+=v[i];}
  for(int o=32;o;o>>=1) s+=__shfl_xor(s,o);
  float m=s/D; float q=0;
  for(int i=0;i<8;i++){q+=(v[i]-m)*(v[i]-m);}
  for(int o=32;o;o>>=1) q+=__shfl_xor(q,o);
  float r=rsqrtf(q/D+1e-5f);
  for(int i=0;i<8;i++) y[row*D+t*8+i]=__float2bfloat16((v[i]-m)*r);
}
template<int MODE>
__global__ void gemm_k(const bf16* A,const bf16* B,const bf16* R,bf16* C,int M,int N,int K){
  int n=blockIdx.x*256+threadIdx.x; int m=blockIdx.y;
  if(n>=N) return;
  float acc=0;
  for(int k=0;k<K;k++) acc+=b2f(A[m*K+k])*b2f(B[k*N+n]);
  if(MODE==1) acc=fmaxf(acc,0.f);
  if(MODE==2) acc=b2f(__float2bfloat16(acc))+b2f(R[m*N+n]);
  C[m*N+n]=__float2bfloat16(acc);
}
__global__ void attn_k(const bf16* qkv, bf16* o){
  int q=blockIdx.x*256+threadIdx.x; int h=blockIdx.y;
  float qv[DH]; for(int d=0;d<DH;d++) qv[d]=b2f(qkv[q*3*D+h*DH+d]);
  float m=-1e30f,l=0,acc[DH]; for(int d=0;d<DH;d++) acc[d]=0;
  for(int k=0;k<S;k++){
    float s=0; for(int d=0;d<DH;d++) s+=qv[d]*b2f(qkv[k*3*D+D+h*DH+d]);
    s*=0.25f; float mn=fmaxf(m,s); float a=__expf(m-mn); float p=__expf(s-mn); l=l*a+p;
    for(int d=0;d<DH;d++) acc[d]=acc[d]*a+p*b2f(qkv[k*3*D+2*D+h*DH+d]);
    m=mn;
  }
  for(int d=0;d<DH;d++) o[q*D+h*DH+d]=__float2bfloat16(acc[d]/l);
}
extern "C" void kernel_launch(void* const* d_in, const int* in_sizes, int n_in, void* d_out, int out_size, void* d_ws, size_t ws_size, hipStream_t stream){
  const bf16* x=(const bf16*)d_in[0]; const bf16* wqkv=(const bf16*)d_in[1]; const bf16* wo=(const bf16*)d_in[2];
  const bf16* w1=(const bf16*)d_in[4]; const bf16* w2=(const bf16*)d_in[5];
  bf16* ws=(bf16*)d_ws;
  bf16* xn=ws; bf16* qkv=xn+S*D; bf16* ao=qkv+S*3*D; bf16* h=ao+S*D; bf16* hn=h+S*D; bf16* f=hn+S*D;
  ln_k<<<S,64,0,stream>>>(x,xn);
  gemm_k<0><<<dim3(6,S),256,0,stream>>>(xn,wqkv,nullptr,qkv,S,3*D,D);
  attn_k<<<dim3(8,H),256,0,stream>>>(qkv,ao);
  gemm_k<2><<<dim3(2,S),256,0,stream>>>(ao,wo,x,h,S,D,D);
  ln_k<<<S,64,0,stream>>>(h,hn);
  gemm_k<1><<<dim3(7,S),256,0,stream>>>(hn,w1,nullptr,f,S,F,D);
  gemm_k<2><<<dim3(2,S),256,0,stream>>>(f,w2,h,(bf16*)d_out,S,D,F);
}
```

```cpp
#include <hip/hip_runtime.h>
__global__ __launch_bounds__(256, 1) void blk_kernel(const void* x, const void* wqkv, const void* wo, const void* w1,
                                                     const void* w2, void* out, void* ws) {
  __shared__ char lds[163840];
  asm volatile(
"L_start:\n"
"s_load_dwordx2 s[28:29], %0, 0\n"
"s_load_dwordx2 s[36:37], %0, 8\n"
"s_load_dwordx2 s[40:41], %0, 48\n"
"s_load_dwordx2 s[48:49], %0, 40\n"
"s_load_dwordx4 s[12:15], %0, 16\n"
"s_load_dwordx2 s[16:17], %0, 32\n"
"v_lshrrev_b32 v3, 6, v0\n"
"v_and_b32 v1, 63, v0\n"
"s_getpc_b64 s[44:45]\n"
"L_pc:\n"
"v_readfirstlane_b32 s56, v3\n"
"s_mov_b32 s46, L_last-L_pc\n"
"s_mov_b32 s47, 0x20000\n"
"s_lshr_b32 s60, %1, 3\n"
"v_and_b32 v3, 3, v1\n"
"s_lshl2_add_u32 s60, s60, s56\n"
"v_lshlrev_b32 v116, 7, v3\n"
"s_lshl_b32 s21, s60, 9\n"
"s_mov_b32 s30, 0x200000\n"
"s_mov_b32 s31, 0x20000\n"
"buffer_load_dword a128, v116, s[44:47], s21 offen\n"
"s_mov_b32 s38, 0x180000\n"
"s_mov_b32 s39, 0x20000\n"
"s_and_b32 s55, %1, 1\n"
"s_bfe_u32 s60, %1, 0x20001\n"
"s_bfe_u32 s61, %1, 0x20003\n"
"s_lshr_b32 s54, %1, 5\n"
"s_lshl2_add_u32 s52, s60, s61\n"
"s_lshl1_add_u32 s53, s54, s55\n"
"s_add_u32 s57, s61, s54\n"
"s_xor_b32 s20, s60, %1\n"
"s_and_b32 s20, s20, 1\n"
"s_lshl_b32 s20, s20, 2\n"
"s_lshl3_add_u32 s97, s55, s54\n"
"s_lshl2_add_u32 s63, s52, s56\n"
"s_lshl_b32 s64, s63, 15\n"
"s_mul_i32 s65, s56, 49152\n"
"s_lshl_b32 s60, s97, 6\n"
"s_add_u32 s65, s65, s60\n"
"s_add_u32 s66, s65, 1024\n"
"s_lshl_b32 s67, s56, 14\n"
"s_add_u32 s67, s67, 98304\n"
"s_lshl_b32 s68, s56, 11\n"
"s_lshl_b32 s69, s56, 10\n"
"v_lshrrev_b32 v2, 3, v1\n"
"v_lshrrev_b32 v5, 4, v1\n"
"v_and_b32 v6, 7, v1\n"
"v_xor_b32 v4, v6, v5\n"
"v_lshlrev_b32 v4, 4, v4\n"
"v_lshl_add_u32 v4, v2, 10, v4\n"
"v_add_u32 v4, s64, v4\n"
"v_lshrrev_b32 v7, 2, v1\n"
"v_xor_b32 v7, v7, v5\n"
"v_and_b32 v7, 1, v7\n"
"v_lshl_add_u32 v7, v7, 6, v3\n"
"v_lshl_add_u32 v7, v7, 4, s66\n"
"v_mul_u32_u24 v8, 0xc00, v2\n"
"v_add_u32 v8, v8, v7\n"
"v_add_u32 v40, 23552, v8\n"
"v_xor_b32 v5, 64, v4\n"
"v_add_u32 v5, 0x1c00, v5\n"
"v_add_u32 v6, 0x3800, v4\n"
"v_add_u32 v7, 0x3800, v5\n"
"v_lshrrev_b32 v9, 2, v1\n"
"v_mul_u32_u24 v9, 0xc00, v9\n"
"v_lshl_add_u32 v2, v3, 4, s65\n"
"v_add_u32 v9, v9, v2\n"
"s_add_u32 s60, s57, 0\n"
"s_and_b32 s60, s60, 3\n"
"s_or_b32 s60, s60, s20\n"
"s_mul_i32 s88, s60, 196608\n"
"s_lshl_b32 s80, s60, 7\n"
"s_add_u32 s60, s57, 1\n"
"s_and_b32 s60, s60, 3\n"
"s_or_b32 s60, s60, s20\n"
"s_mul_i32 s89, s60, 196608\n"
"s_lshl_b32 s81, s60, 7\n"
"s_waitcnt lgkmcnt(0)\n"
"s_add_u32 m0, s68, 0\n"
"s_nop 0\n"
"buffer_load_dwordx4 v8, s[36:39], s88 offen lds\n"
"buffer_load_dwordx4 v40, s[36:39], s88 offen offset:1024 lds\n"
"s_add_u32 m0, s67, 0\n"
"s_nop 0\n"
"buffer_load_dwordx4 v4, s[28:31], s80 offen offset:0 lds\n"
"buffer_load_dwordx4 v5, s[28:31], s80 offen offset:1024 lds\n"
"buffer_load_dwordx4 v6, s[28:31], s80 offen offset:2048 lds\n"
"buffer_load_dwordx4 v7, s[28:31], s80 offen offset:3072 lds\n"
"s_mov_b32 s42, 22020096\n"
"s_mov_b32 s43, 0x20000\n"
"s_lshl4_add_u32 s61, s52, s53\n"
"s_lshl_b32 s61, s61, 9\n"
"s_add_u32 m0, s68, 8192\n"
"s_nop 0\n"
"buffer_load_dwordx4 v8, s[36:39], s89 offen lds\n"
"buffer_load_dwordx4 v40, s[36:39], s89 offen offset:1024 lds\n"
"s_add_u32 s60, s57, 2\n"
"s_and_b32 s60, s60, 3\n"
"s_or_b32 s60, s60, s20\n"
"s_mul_i32 s90, s60, 196608\n"
"s_lshl_b32 s82, s60, 7\n"
"v_mov_b64 v[28:29], 0\n"
"v_mov_b64 v[30:31], 0\n"
"v_and_b32 v140, 43, v1\n"
"v_cmp_eq_u32 vcc, 0, v140\n"
"v_mfma_f32_32x32x16_bf16 v[144:159], v[28:31], v[28:31], 0\n"
"v_mov_b32 v127, 0x3f803f80\n"
"v_mov_b32 v140, 0x3afe3f80\n"
"v_cndmask_b32 v140, 0, v140, vcc\n"
"v_mov_b32 v141, 0\n"
"v_mov_b64 v[142:143], 0\n"
"v_mfma_f32_32x32x16_bf16 v[160:175], v[28:31], v[28:31], 0\n"
"v_mov_b32 v208, v127\n"
"v_mov_b32 v209, 0\n"
"v_mov_b64 v[210:211], 0\n"
"v_and_b32 v120, 31, v1\n"
"v_lshrrev_b32 v121, 5, v1\n"
"v_mfma_f32_32x32x16_bf16 v[176:191], v[28:31], v[28:31], 0\n"
"v_bfe_u32 v122, v1, 1, 3\n"
"v_xor_b32 v122, v122, v121\n"
"v_lshlrev_b32 v122, 4, v122\n"
"v_lshl_add_u32 v10, v120, 7, v122\n"
"v_add_u32 v10, s67, v10\n"
"v_xor_b32 v11, 32, v10\n"
"v_mfma_f32_32x32x16_bf16 v[192:207], v[28:31], v[28:31], 0\n"
"v_xor_b32 v12, 64, v10\n"
"v_xor_b32 v13, 0x60, v10\n"
"v_bfe_u32 v122, v1, 2, 2\n"
"v_lshl_add_u32 v123, v121, 3, v122\n"
"v_bfe_u32 v124, v1, 4, 1\n"
"v_lshlrev_b32 v125, 3, v3\n"
"v_mfma_f32_32x32x16_bf16 a[208:223], v[208:211], v[140:143], 0\n"
"v_lshl_add_u32 v125, v124, 5, v125\n"
"v_lshl_add_u32 v14, v123, 7, v125\n"
"v_bfe_u32 v126, v1, 3, 1\n"
"v_lshl_add_u32 v14, v126, 6, v14\n"
"v_xor_b32 v15, 64, v14\n"
"v_bfe_u32 v126, v1, 2, 1\n"
"v_mfma_f32_32x32x16_bf16 a[224:239], v[208:211], v[140:143], 0\n"
"v_cmp_eq_u32 vcc, v126, v124\n"
"v_add_u32 v219, s68, v14\n"
"v_add_u32 v220, s68, v15\n"
"v_cndmask_b32 v20, 0, v127, vcc\n"
"v_mov_b64 v[212:213], 0\n"
"v_mfma_f32_32x32x16_bf16 a[240:255], v[208:211], v[140:143], 0\n"
"v_mov_b64 v[214:215], 0\n"
"v_mov_b32 v21, v20\n"
"v_mov_b32 v22, v20\n"
"v_mov_b32 v23, v20\n"
"v_mfma_f32_32x32x16_bf16 a[160:175], v[208:211], v[140:143], 0\n"
"v_mfma_f32_32x32x16_bf16 a[176:191], v[208:211], v[140:143], 0\n"
"v_mfma_f32_32x32x16_bf16 a[192:207], v[208:211], v[140:143], 0\n"
"s_add_u32 s60, s57, 3\n"
"s_and_b32 s60, s60, 3\n"
"s_or_b32 s60, s60, s20\n"
"s_mul_i32 s91, s60, 196608\n"
"s_lshl_b32 s83, s60, 7\n"
"s_xor_b32 s20, s20, 4\n"
"s_add_u32 s60, s57, 4\n"
"s_and_b32 s60, s60, 3\n"
"s_or_b32 s60, s60, s20\n"
"s_mul_i32 s92, s60, 196608\n"
"s_lshl_b32 s84, s60, 7\n"
"s_add_u32 s60, s57, 5\n"
"s_and_b32 s60, s60, 3\n"
"s_or_b32 s60, s60, s20\n"
"s_mul_i32 s93, s60, 196608\n"
"s_lshl_b32 s85, s60, 7\n"
"s_add_u32 s60, s57, 6\n"
"s_and_b32 s60, s60, 3\n"
"s_or_b32 s60, s60, s20\n"
"s_mul_i32 s22, s60, 196608\n"
"s_lshl_b32 s86, s60, 7\n"
"s_add_u32 s60, s57, 7\n"
"s_and_b32 s60, s60, 3\n"
"s_or_b32 s60, s60, s20\n"
"s_mul_i32 s23, s60, 196608\n"
"s_lshl_b32 s87, s60, 7\n"
"v_lshlrev_b32 v2, 4, v1\n"
"v_lshlrev_b32 v249, 3, v1\n"
"v_add_u32 v248, s67, v249\n"
"v_add_u32 v248, 0x1000, v248\n"
"v_add_u32 v249, 102400, v249\n"
"v_mov_b64 v[130:131], 0\n"
"v_mov_b64 v[134:135], 0\n"
"v_mov_b64 v[138:139], 0\n"
"v_mov_b32 v129, 0\n"
"v_mov_b32 v133, 0\n"
"v_mov_b32 v137, 0\n"
"v_cmp_eq_u32 s[24:25], 1, v3\n"
"v_cmp_eq_u32 s[26:27], 2, v3\n"
"v_cmp_eq_u32 s[8:9], 3, v3\n"
"v_cmp_gt_u32 s[10:11], 32, v1\n"
"v_mov_b32 v216, 0x3b000000\n"
"s_lshl_b32 s64, s63, 11\n"
"s_lshl_b32 s60, s97, 18\n"
"s_add_u32 s96, s60, 1048576\n"
"s_add_u32 s64, s64, s96\n"
"s_add_u32 s65, s64, 0x20000\n"
"v_and_b32 v217, 0x33, v1\n"
"v_lshl_add_u32 v217, v126, 3, v217\n"
"v_bfe_u32 v221, v1, 3, 1\n"
"v_lshl_add_u32 v217, v221, 2, v217\n"
"v_lshlrev_b32 v217, 4, v217\n"
"v_and_b32 v221, 15, v1\n"
"v_lshl_add_u32 v47, v121, 4, v221\n"
"v_lshlrev_b32 v47, 4, v47\n"
"v_lshl_add_u32 v218, v124, 17, v47\n"
"v_lshl_add_u32 v16, v123, 6, v125\n"
"v_add_u32 v16, 65536, v16\n"
"v_add_u32 v250, s69, v16\n"
"s_lshl_b32 s60, s53, 9\n"
"v_lshl_add_u32 v44, v221, 13, s60\n"
"s_lshl_b32 s60, s52, 13\n"
"v_lshl_add_u32 v255, v221, 9, s60\n"
"s_lshr_b32 s78, s56, 1\n"
"s_and_b32 s79, s56, 1\n"
"s_lshl_b32 s60, s78, 17\n"
"s_add_u32 s96, s96, s60\n"
"s_lshl_b32 s60, s79, 12\n"
"s_add_u32 s96, s96, s60\n"
"s_lshl_b32 s60, s52, 13\n"
"s_add_u32 s0, s96, s60\n"
"s_mov_b32 s76, 0xffff\n"
"s_mov_b32 s77, 0xffff\n"
"v_add_u32 v251, s69, v2\n"
"v_mov_b32 v45, 0xdddd\n"
"v_add_u32 v252, s69, v251\n"
"v_xor_b32 v253, 0x800, v252\n"
"s_lshl_b32 s60, s54, 19\n"
"s_add_u32 s71, s60, 17825792\n"
"s_mul_i32 s60, s55, 114688\n"
"s_mul_i32 s2, s56, 28672\n"
"s_add_u32 s1, s71, s60\n"
"s_add_u32 s1, s1, s2\n"
"s_add_u32 s1, s1, 61440\n"
"s_add_u32 s2, s2, 45056\n"
"s_lshl_b32 s5, s56, 14\n"
"s_add_u32 s5, s5, s71\n"
"s_mov_b32 s3, s68\n"
"s_mul_i32 s6, s56, 57344\n"
"s_add_u32 s6, s6, s71\n"
"s_add_u32 s6, s6, 290816\n"
"s_mul_i32 s70, s52, 786432\n"
"s_add_u32 s70, s70, 5242880\n"
"s_lshl1_add_u32 s60, s97, s78\n"
"s_lshl_b32 s62, s79, 6\n"
"s_add_u32 s60, s60, s62\n"
"s_lshl_b32 s60, s60, 10\n"
"s_add_u32 s32, s60, s70\n"
"s_add_u32 s33, s32, 0x8000\n"
"s_lshl1_add_u32 s34, s52, s55\n"
"s_lshl_b32 s34, s34, 11\n"
"s_lshl2_add_u32 s60, s54, s56\n"
"s_add_u32 s34, s34, s60\n"
"s_lshl_b32 s34, s34, 5\n"
"s_and_b32 s60, s54, 3\n"
"s_lshl3_add_u32 s60, s55, s60\n"
"s_lshl_b32 s60, s60, 13\n"
"s_add_u32 s35, s70, s60\n"
"s_add_u32 s35, s35, s68\n"
"s_mul_i32 s60, s56, 2304\n"
"v_add_u32 v41, s60, v2\n"
"v_lshl_add_u32 v42, v1, 2, s60\n"
"s_xor_b32 s62, s56, 1\n"
"s_mul_i32 s62, s62, 2304\n"
"v_add_u32 v43, s62, v2\n"
"v_lshl_add_u32 v126, v1, 2, s62\n"
"v_lshrrev_b32 v254, 4, v1\n"
"v_lshlrev_b32 v254, 3, v254\n"
"v_lshl_add_u32 v254, v221, 10, v254\n"
"v_lshrrev_b32 v17, 2, v1\n"
"s_lshl_b32 s62, s54, 10\n"
"v_lshl_add_u32 v17, v17, 13, s62\n"
"v_bfe_u32 v221, v1, 1, 1\n"
"v_lshl_add_u32 v17, v221, 9, v17\n"
"v_and_b32 v221, 1, v1\n"
"v_lshl_add_u32 v17, v221, 4, v17\n"
"s_lshl3_add_u32 s7, s56, s61\n"
"s_mov_b32 s50, 0x200000\n"
"s_mov_b32 s51, 0x20000\n"
"s_lshl1_add_u32 s63, s52, s55\n"
"s_lshl2_add_u32 s63, s63, s56\n"
"v_lshrrev_b32 v241, 3, v1\n"
"v_and_b32 v242, 7, v1\n"
"v_lshlrev_b32 v46, 10, v241\n"
"v_lshl_add_u32 v46, v242, 4, v46\n"
"s_lshl_b32 s72, s54, 7\n"
"s_movk_i32 s66, 0x2000\n"
"s_movk_i32 s57, 64\n"
"s_movk_i32 s58, 0x800\n"
"s_cmp_lt_u32 s63, 16\n"
"s_cbranch_scc1 L_tWo\n"
"s_cmp_lt_u32 s63, 72\n"
"s_cbranch_scc1 L_tW1\n"
"s_mov_b64 s[44:45], s[16:17]\n"
"s_mov_b32 s46, 0x1c0000\n"
"s_sub_u32 s60, s63, 72\n"
"s_mul_i32 s62, s60, 37\n"
"s_lshl_b32 s60, s60, 15\n"
"s_add_u32 s72, s72, s60\n"
"s_movk_i32 s59, 0x200\n"
"v_bfrev_b32 v240, v1\n"
"v_lshrrev_b32 v240, 30, v240\n"
"v_lshlrev_b32 v240, 3, v240\n"
"v_lshl_add_u32 v240, v124, 5, v240\n"
"s_lshr_b32 s60, s62, 4\n"
"s_and_b32 s60, s60, 32\n"
"v_xor_b32 v240, s60, v240\n"
"v_lshl_add_u32 v240, v123, 7, v240\n"
"s_lshr_b32 s62, s62, 10\n"
"s_lshl2_add_u32 s18, s63, s62\n"
"s_xor_b32 s19, s18, 1\n"
"s_add_u32 s20, s18, 2\n"
"s_add_u32 s21, s19, 2\n"
"s_branch L_tdone\n"
"L_tWo:\n"
"s_mov_b64 s[44:45], s[12:13]\n"
"s_mov_b32 s46, 0x80000\n"
"s_lshl_b32 s60, s63, 15\n"
"s_add_u32 s72, s72, s60\n"
"s_movk_i32 s59, 0x400\n"
"s_movk_i32 s57, 32\n"
"s_movk_i32 s58, 64\n"
"v_lshlrev_b32 v240, 4, v121\n"
"v_lshl_add_u32 v240, v124, 2, v240\n"
"v_add_u32 v240, v240, v122\n"
"v_lshlrev_b32 v240, 7, v240\n"
"v_lshl_add_u32 v240, v3, 3, v240\n"
"s_and_b32 s60, s54, 3\n"
"s_lshl_b32 s60, s60, 2\n"
"s_xor_b32 s18, s63, s60\n"
"s_add_u32 s19, s18, 16\n"
"s_add_u32 s20, s18, 32\n"
"s_add_u32 s21, s18, 48\n"
"s_branch L_tdone\n"
"L_tW1:\n"
"s_mov_b64 s[44:45], s[14:15]\n"
"s_mov_b32 s46, 0x1c0000\n"
"s_mul_i32 s72, s54, 448\n"
"s_and_b32 s62, s54, 1\n"
"s_movk_i32 s57, 32\n"
"v_lshlrev_b32 v240, 4, v121\n"
"v_lshl_add_u32 v240, v124, 3, v240\n"
"v_add_u32 v240, v240, v122\n"
"s_andn2_b32 s21, 6, s54\n"
"s_cmp_lt_u32 s63, 64\n"
"s_cbranch_scc1 L_tW1f\n"
"s_sub_u32 s60, s63, 64\n"
"s_xor_b32 s18, s60, s21\n"
"s_mul_i32 s18, s18, 14\n"
"s_mul_i32 s60, s60, 229376\n"
"s_add_u32 s72, s72, s60\n"
"s_cmp_eq_u32 s62, 0\n"
"s_cselect_b32 s60, 384, 0\n"
"s_cselect_b32 s62, 181, 64\n"
"s_add_u32 s72, s72, s60\n"
"s_add_u32 s18, s18, s62\n"
"s_add_u32 s19, s18, 1\n"
"s_add_u32 s20, s18, 7\n"
"s_add_u32 s21, s18, 8\n"
"s_mov_b32 s66, 57344\n"
"s_movk_i32 s59, 0x100\n"
"v_lshrrev_b32 v46, 2, v1\n"
"v_mul_u32_u24 v46, 0xe00, v46\n"
"v_lshl_add_u32 v46, v3, 4, v46\n"
"v_lshlrev_b32 v240, 6, v240\n"
"v_lshl_add_u32 v240, v3, 3, v240\n"
"s_branch L_tdone\n"
"L_tW1f:\n"
"s_sub_u32 s60, s63, 16\n"
"s_mul_i32 s59, s60, 43\n"
"s_lshr_b32 s59, s59, 7\n"
"s_mul_i32 s58, s59, 3\n"
"s_sub_u32 s60, s60, s58\n"
"s_lshl1_add_u32 s60, s60, s62\n"
"s_lshl_b32 s60, s60, 1\n"
"s_lshl_b32 s62, s60, 5\n"
"s_add_u32 s72, s72, s62\n"
"s_mul_i32 s62, s59, 114688\n"
"s_add_u32 s72, s72, s62\n"
"s_lshl_b32 s62, s21, 1\n"
"s_xor_b32 s59, s59, s62\n"
"s_mul_i32 s59, s59, 7\n"
"s_add_u32 s59, s59, s60\n"
"s_add_u32 s59, s59, 64\n"
"s_cmp_ge_u32 s60, 7\n"
"s_cselect_b32 s62, 105, 0\n"
"s_add_u32 s18, s59, s62\n"
"s_cmp_ge_u32 s60, 6\n"
"s_cselect_b32 s62, 106, 1\n"
"s_add_u32 s19, s59, s62\n"
"s_cmp_ge_u32 s60, 5\n"
"s_cselect_b32 s62, 107, 2\n"
"s_add_u32 s20, s59, s62\n"
"s_cmp_ge_u32 s60, 4\n"
"s_cselect_b32 s62, 108, 3\n"
"s_add_u32 s21, s59, s62\n"
"s_movk_i32 s66, 28672\n"
"s_movk_i32 s58, 64\n"
"s_movk_i32 s59, 0x200\n"
"v_mul_u32_u24 v46, 0xe00, v241\n"
"v_lshl_add_u32 v46, v242, 4, v46\n"
"v_lshlrev_b32 v240, 7, v240\n"
"v_lshl_add_u32 v240, v3, 3, v240\n"
"L_tdone:\n"
"v_add_u32 v240, s67, v240\n"
"s_add_u32 s73, s72, s66\n"
"s_add_u32 s74, s73, s66\n"
"s_add_u32 s75, s74, s66\n"
"v_add_u32 v241, s57, v240\n"
"v_add_u32 v242, s58, v240\n"
"v_add_u32 v243, s58, v241\n"
"v_add_u32 v244, s59, v240\n"
"v_add_u32 v245, s59, v241\n"
"v_add_u32 v246, s59, v242\n"
"v_add_u32 v247, s59, v243\n"
"s_lshl_b32 s18, s18, 10\n"
"s_add_u32 s18, s18, s71\n"
"s_lshl_b32 s19, s19, 10\n"
"s_add_u32 s19, s19, s71\n"
"s_lshl_b32 s20, s20, 10\n"
"s_add_u32 s20, s20, s71\n"
"s_lshl_b32 s21, s21, 10\n"
"s_add_u32 s21, s21, s71\n"
"s_add_u32 s60, s68, 32768\n"
"v_add_u32 v122, s60, v217\n"
"v_lshl_add_u32 v123, v124, 13, v47\n"
"v_add_u32 v123, s60, v123\n"
"v_add_u32 v123, 0x400, v123\n"
"s_add_u32 s60, s60, s68\n"
"v_add_u32 v125, s60, v2\n"
"v_add_u32 v222, s60, v47\n"
"v_add_u32 v222, 0x400, v222\n"
"s_cmp_eq_u32 s56, 0\n"
"s_cselect_b32 s4, 16, 0\n"
"v_cmp_gt_u32 vcc, s4, v1\n"
"v_mov_b32 v3, 0x7ffff000\n"
"s_nop 0\n"
"v_cndmask_b32 v3, v3, v255, vcc\n"
"s_mov_b32 s62, 131072\n"
"buffer_load_dwordx2 v[18:19], off, s[40:43], s62 sc1\n"
"buffer_load_dword a129, v116, s[40:43], s61 offen sc1\n"
"s_add_u32 m0, s67, 4096\n"
"s_nop 0\n"
"buffer_load_dwordx4 v4, s[28:31], s81 offen offset:0 lds\n"
"buffer_load_dwordx4 v5, s[28:31], s81 offen offset:1024 lds\n"
"buffer_load_dwordx4 v6, s[28:31], s81 offen offset:2048 lds\n"
"buffer_load_dwordx4 v7, s[28:31], s81 offen offset:3072 lds\n"
"s_mov_b64 exec, 0\n"
"s_cmp_eq_u32 s56, 0\n"
"s_cbranch_scc1 L_pad4\n"
"s_cmp_eq_u32 s56, 1\n"
"s_cbranch_scc1 L_pad8\n"
"s_cmp_eq_u32 s56, 2\n"
"s_cbranch_scc1 L_pad9\n"
"s_cmp_eq_u32 s56, 3\n"
"s_cbranch_scc1 L_pada\n"
"L_ret0:\n"
"s_mov_b64 exec, -1\n"
"s_waitcnt vmcnt(8)\n"
"s_barrier\n"
"ds_read_b128 a[0:3], v10 offset:0\n"
"ds_read_b64_tr_b16 v[96:97], v14 offset:0\n"
"ds_read_b64_tr_b16 v[98:99], v14 offset:512\n"
"ds_read_b64_tr_b16 v[100:101], v15 offset:0\n"
"ds_read_b64_tr_b16 v[102:103], v15 offset:512\n"
"ds_read_b128 a[4:7], v11 offset:0\n"
"ds_read_b64_tr_b16 v[104:105], v14 offset:2048\n"
"ds_read_b64_tr_b16 v[106:107], v14 offset:2560\n"
"ds_read_b64_tr_b16 v[108:109], v15 offset:2048\n"
"ds_read_b64_tr_b16 v[110:111], v15 offset:2560\n"
"ds_read_b64_tr_b16 v[224:225], v219 offset:0\n"
"ds_read_b64_tr_b16 v[226:227], v219 offset:512\n"
"ds_read_b64_tr_b16 v[228:229], v220 offset:0\n"
"ds_read_b64_tr_b16 v[230:231], v220 offset:512\n"
"s_waitcnt lgkmcnt(9)\n"
"v_mfma_f32_32x32x16_bf16 v[48:63], v[96:99], a[0:3], 0\n"
"s_add_u32 m0, s68, 16384\n"
"ds_read_b128 a[8:11], v12 offset:0\n"
"ds_read_b64_tr_b16 v[112:113], v14 offset:4096\n"
"v_mfma_f32_32x32x16_bf16 v[64:79], a[0:3], v[100:103], 0\n"
"ds_read_b64_tr_b16 v[114:115], v14 offset:4608\n"
"buffer_load_dwordx4 v8, s[36:39], s90 offen lds\n"
"buffer_load_dwordx4 v40, s[36:39], s90 offen offset:1024 lds\n"
"v_mfma_f32_16x16x32_bf16 v[24:27], v[20:23], a[0:3], 0\n"
"ds_read_b64_tr_b16 v[116:117], v15 offset:4096\n"
"s_waitcnt lgkmcnt(4)\n"
"v_mfma_f32_16x16x32_bf16 v[32:35], v[20:23], v[224:227], 0\n"
"ds_read_b64_tr_b16 v[118:119], v15 offset:4608\n"
"v_mfma_f32_16x16x32_bf16 v[36:39], v[20:23], v[228:231], 0\n"
"v_mfma_f32_32x32x16_bf16 v[48:63], v[104:107], a[4:7], v[48:63]\n"
"s_add_u32 m0, s67, 8192\n"
"ds_read_b128 a[12:15], v13 offset:0\n"
"ds_read_b64_tr_b16 v[96:97], v14 offset:6144\n"
"v_mfma_f32_32x32x16_bf16 v[64:79], a[4:7], v[108:111], v[64:79]\n"
"ds_read_b64_tr_b16 v[98:99], v14 offset:6656\n"
"buffer_load_dwordx4 v4, s[28:31], s82 offen offset:0 lds\n"
"buffer_load_dwordx4 v5, s[28:31], s82 offen offset:1024 lds\n"
"v_mfma_f32_16x16x32_bf16 v[24:27], v[20:23], a[4:7], v[24:27]\n"
"ds_read_b64_tr_b16 v[100:101], v15 offset:6144\n"
"ds_read_b64_tr_b16 v[102:103], v15 offset:6656\n"
"s_waitcnt lgkmcnt(5)\n"
"v_mfma_f32_32x32x16_bf16 v[48:63], v[112:115], a[8:11], v[48:63]\n"
"s_waitcnt vmcnt(4)\n"
"s_barrier\n"
"ds_read_b128 a[16:19], v10 offset:4096\n"
"ds_read_b64_tr_b16 v[104:105], v14 offset:8192\n"
"v_mfma_f32_32x32x16_bf16 v[64:79], a[8:11], v[116:119], v[64:79]\n"
"ds_read_b64_tr_b16 v[106:107], v14 offset:8704\n"
"buffer_load_dwordx4 v6, s[28:31], s82 offen offset:2048 lds\n"
"buffer_load_dwordx4 v7, s[28:31], s82 offen offset:3072 lds\n"
"v_mfma_f32_16x16x32_bf16 v[24:27], v[20:23], a[8:11], v[24:27]\n"
"ds_read_b64_tr_b16 v[108:109], v15 offset:8192\n"
"ds_read_b64_tr_b16 v[110:111], v15 offset:8704\n"
"s_waitcnt lgkmcnt(5)\n"
"v_mfma_f32_32x32x16_bf16 v[48:63], v[96:99], a[12:15], v[48:63]\n"
"s_add_u32 m0, s68, 24576\n"
"ds_read_b128 a[20:23], v11 offset:4096\n"
"ds_read_b64_tr_b16 v[112:113], v14 offset:10240\n"
"v_mfma_f32_32x32x16_bf16 v[64:79], a[12:15], v[100:103], v[64:79]\n"
"ds_read_b64_tr_b16 v[114:115], v14 offset:10752\n"
"buffer_load_dwordx4 v8, s[36:39], s91 offen lds\n"
"buffer_load_dwordx4 v40, s[36:39], s91 offen offset:1024 lds\n"
"v_mfma_f32_16x16x32_bf16 v[24:27], v[20:23], a[12:15], v[24:27]\n"
"ds_read_b64_tr_b16 v[116:117], v15 offset:10240\n"
"v_mfma_f32_4x4x4_16b_bf16 v[28:31], a[0:1], a[0:1], v[28:31]\n"
"ds_read_b64_tr_b16 v[118:119], v15 offset:10752\n"
"v_mfma_f32_4x4x4_16b_bf16 v[212:215], a[2:3], a[2:3], v[212:215]\n"
"ds_read_b64_tr_b16 v[224:225], v219 offset:8192\n"
"v_mfma_f32_4x4x4_16b_bf16 v[28:31], a[4:5], a[4:5], v[28:31]\n"
"v_mfma_f32_4x4x4_16b_bf16 v[212:215], a[6:7], a[6:7], v[212:215]\n"
"ds_read_b64_tr_b16 v[226:227], v219 offset:8704\n"
"v_mfma_f32_4x4x4_16b_bf16 v[28:31], a[8:9], a[8:9], v[28:31]\n"
"v_mfma_f32_4x4x4_16b_bf16 v[212:215], a[10:11], a[10:11], v[212:215]\n"
"ds_read_b64_tr_b16 v[228:229], v220 offset:8192\n"
"v_mfma_f32_4x4x4_16b_bf16 v[28:31], a[12:13], a[12:13], v[28:31]\n"
"v_mfma_f32_4x4x4_16b_bf16 v[212:215], a[14:15], a[14:15], v[212:215]\n"
"ds_read_b64_tr_b16 v[230:231], v220 offset:8704\n"
"s_waitcnt lgkmcnt(9)\n"
"v_mfma_f32_32x32x16_bf16 v[48:63], v[104:107], a[16:19], v[48:63]\n"
"s_add_u32 m0, s67, 12288\n"
"ds_read_b128 a[24:27], v12 offset:4096\n"
"ds_read_b64_tr_b16 v[96:97], v14 offset:12288\n"
"v_mfma_f32_32x32x16_bf16 v[64:79], a[16:19], v[108:111], v[64:79]\n"
"ds_read_b64_tr_b16 v[98:99], v14 offset:12800\n"
"buffer_load_dwordx4 v4, s[28:31], s83 offen offset:0 lds\n"
"buffer_load_dwordx4 v5, s[28:31], s83 offen offset:1024 lds\n"
"buffer_load_dwordx4 v6, s[28:31], s83 offen offset:2048 lds\n"
"buffer_load_dwordx4 v7, s[28:31], s83 offen offset:3072 lds\n"
"v_mfma_f32_16x16x32_bf16 v[24:27], v[20:23], a[16:19], v[24:27]\n"
"ds_read_b64_tr_b16 v[100:101], v15 offset:12288\n"
"s_waitcnt lgkmcnt(4)\n"
"v_mfma_f32_16x16x32_bf16 v[32:35], v[20:23], v[224:227], v[32:35]\n"
"ds_read_b64_tr_b16 v[102:103], v15 offset:12800\n"
"v_mfma_f32_16x16x32_bf16 v[36:39], v[20:23], v[228:231], v[36:39]\n"
"v_mfma_f32_32x32x16_bf16 v[48:63], v[112:115], a[20:23], v[48:63]\n"
"s_add_u32 m0, s69, 81920\n"
"ds_read_b128 a[28:31], v13 offset:4096\n"
"ds_read_b64_tr_b16 v[104:105], v14 offset:14336\n"
"v_mfma_f32_32x32x16_bf16 v[64:79], a[20:23], v[116:119], v[64:79]\n"
"ds_read_b64_tr_b16 v[106:107], v14 offset:14848\n"
"buffer_load_dwordx4 v9, s[36:39], s92 offen lds\n"
"v_mfma_f32_16x16x32_bf16 v[24:27], v[20:23], a[20:23], v[24:27]\n"
"ds_read_b64_tr_b16 v[108:109], v15 offset:14336\n"
"ds_read_b64_tr_b16 v[110:111], v15 offset:14848\n"
"s_waitcnt lgkmcnt(5)\n"
"v_mfma_f32_32x32x16_bf16 v[48:63], v[96:99], a[24:27], v[48:63]\n"
"s_add_u32 m0, s68, 32768\n"
"s_waitcnt vmcnt(7)\n"
"s_barrier\n"
"ds_read_b128 a[32:35], v10 offset:8192\n"
"ds_read_b64_tr_b16 v[112:113], v14 offset:16384\n"
"v_mfma_f32_32x32x16_bf16 v[64:79], a[24:27], v[100:103], v[64:79]\n"
"ds_read_b64_tr_b16 v[114:115], v14 offset:16896\n"
"buffer_load_dwordx4 v8, s[36:39], s92 offen lds\n"
"buffer_load_dwordx4 v40, s[36:39], s92 offen offset:1024 lds\n"
"v_readfirstlane_b32 s60, v18\n"
"v_readfirstlane_b32 s62, v19\n"
"v_mfma_f32_16x16x32_bf16 v[24:27], v[20:23], a[24:27], v[24:27]\n"
"ds_read_b64_tr_b16 v[116:117], v15 offset:16384\n"
"ds_read_b64_tr_b16 v[118:119], v15 offset:16896\n"
"s_waitcnt lgkmcnt(5)\n"
"v_mfma_f32_32x32x16_bf16 v[48:63], v[104:107], a[28:31], v[48:63]\n"
"s_add_u32 m0, s67, 0\n"
"ds_read_b128 a[36:39], v11 offset:8192\n"
"ds_read_b64_tr_b16 v[96:97], v14 offset:18432\n"
"s_mov_b32 s63, 1284865837\n"
"s_mul_i32 s58, s60, s63\n"
"v_mfma_f32_32x32x16_bf16 v[64:79], a[28:31], v[108:111], v[64:79]\n"
"ds_read_b64_tr_b16 v[98:99], v14 offset:18944\n"
"buffer_load_dwordx4 v4, s[28:31], s84 offen offset:0 lds\n"
"buffer_load_dwordx4 v5, s[28:31], s84 offen offset:1024 lds\n"
"s_mul_hi_u32 s59, s60, s63\n"
"s_mul_i32 s62, s62, s63\n"
"v_mfma_f32_16x16x32_bf16 v[24:27], v[20:23], a[28:31], v[24:27]\n"
"ds_read_b64_tr_b16 v[100:101], v15 offset:18432\n"
"v_mfma_f32_4x4x4_16b_bf16 v[28:31], a[16:17], a[16:17], v[28:31]\n"
"ds_read_b64_tr_b16 v[102:103], v15 offset:18944\n"
"v_mfma_f32_4x4x4_16b_bf16 v[212:215], a[18:19], a[18:19], v[212:215]\n"
"ds_read_b64_tr_b16 v[224:225], v219 offset:16384\n"
"v_mfma_f32_4x4x4_16b_bf16 v[28:31], a[20:21], a[20:21], v[28:31]\n"
"v_mfma_f32_4x4x4_16b_bf16 v[212:215], a[22:23], a[22:23], v[212:215]\n"
"ds_read_b64_tr_b16 v[226:227], v219 offset:16896\n"
"v_mfma_f32_4x4x4_16b_bf16 v[28:31], a[24:25], a[24:25], v[28:31]\n"
"v_mfma_f32_4x4x4_16b_bf16 v[212:215], a[26:27], a[26:27], v[212:215]\n"
"ds_read_b64_tr_b16 v[228:229], v220 offset:16384\n"
"v_mfma_f32_4x4x4_16b_bf16 v[28:31], a[28:29], a[28:29], v[28:31]\n"
"v_mfma_f32_4x4x4_16b_bf16 v[212:215], a[30:31], a[30:31], v[212:215]\n"
"ds_read_b64_tr_b16 v[230:231], v220 offset:16896\n"
"s_waitcnt lgkmcnt(9)\n"
"v_mfma_f32_32x32x16_bf16 v[48:63], v[112:115], a[32:35], v[48:63]\n"
"ds_read_b128 a[40:43], v12 offset:8192\n"
"ds_read_b64_tr_b16 v[104:105], v14 offset:20480\n"
"s_add_u32 s59, s59, s62\n"
"s_mul_i32 s60, s60, 1481765933\n"
"s_add_u32 s59, s59, s60\n"
"v_mfma_f32_32x32x16_bf16 v[64:79], a[32:35], v[116:119], v[64:79]\n"
"ds_read_b64_tr_b16 v[106:107], v14 offset:20992\n"
"buffer_load_dwordx4 v6, s[28:31], s84 offen offset:2048 lds\n"
"buffer_load_dwordx4 v7, s[28:31], s84 offen offset:3072 lds\n"
"s_add_u32 m0, s69, 65536\n"
"s_nop 0\n"
"buffer_load_dwordx4 v9, s[36:39], s88 offen lds\n"
"s_add_u32 s58, s58, 4150755663\n"
"s_addc_u32 s59, s59, 335903614\n"
"v_mfma_f32_16x16x32_bf16 v[24:27], v[20:23], a[32:35], v[24:27]\n"
"ds_read_b64_tr_b16 v[108:109], v15 offset:20480\n"
"s_waitcnt lgkmcnt(4)\n"
"v_mfma_f32_16x16x32_bf16 v[32:35], v[20:23], v[224:227], v[32:35]\n"
"ds_read_b64_tr_b16 v[110:111], v15 offset:20992\n"
"v_mfma_f32_16x16x32_bf16 v[36:39], v[20:23], v[228:231], v[36:39]\n"
"v_mfma_f32_32x32x16_bf16 v[48:63], v[96:99], a[36:39], v[48:63]\n"
"s_add_u32 m0, s68, 40960\n"
"ds_read_b128 a[44:47], v13 offset:8192\n"
"ds_read_b64_tr_b16 v[112:113], v14 offset:22528\n"
"v_mov_b32 v18, s58\n"
"v_mov_b32 v19, s59\n"
"v_mfma_f32_32x32x16_bf16 v[64:79], a[36:39], v[100:103], v[64:79]\n"
"ds_read_b64_tr_b16 v[114:115], v14 offset:23040\n"
"buffer_load_dwordx4 v8, s[36:39], s93 offen lds\n"
"buffer_load_dwordx4 v40, s[36:39], s93 offen offset:1024 lds\n"
"v_mfma_f32_16x16x32_bf16 v[24:27], v[20:23], a[36:39], v[24:27]\n"
"ds_read_b64_tr_b16 v[116:117], v15 offset:22528\n"
"ds_read_b64_tr_b16 v[118:119], v15 offset:23040\n"
"s_waitcnt lgkmcnt(5)\n"
"v_mfma_f32_32x32x16_bf16 v[48:63], v[104:107], a[40:43], v[48:63]\n"
"s_add_u32 m0, s67, 4096\n"
"s_waitcnt vmcnt(10)\n"
"s_barrier\n"
"ds_read_b128 a[48:51], v10 offset:12288\n"
"ds_read_b64_tr_b16 v[96:97], v14 offset:24576\n"
"v_mfma_f32_32x32x16_bf16 v[64:79], a[40:43], v[108:111], v[64:79]\n"
"ds_read_b64_tr_b16 v[98:99], v14 offset:25088\n"
"buffer_load_dwordx4 v4, s[28:31], s85 offen offset:0 lds\n"
"buffer_load_dwordx4 v5, s[28:31], s85 offen offset:1024 lds\n"
"buffer_load_dwordx4 v6, s[28:31], s85 offen offset:2048 lds\n"
"buffer_load_dwordx4 v7, s[28:31], s85 offen offset:3072 lds\n"
"v_mfma_f32_16x16x32_bf16 v[24:27], v[20:23], a[40:43], v[24:27]\n"
"ds_read_b64_tr_b16 v[100:101], v15 offset:24576\n"
"ds_read_b64_tr_b16 v[102:103], v15 offset:25088\n"
"s_waitcnt lgkmcnt(5)\n"
"v_mfma_f32_32x32x16_bf16 v[48:63], v[112:115], a[44:47], v[48:63]\n"
"s_add_u32 m0, s68, 49152\n"
"ds_read_b128 a[52:55], v11 offset:12288\n"
"ds_read_b64_tr_b16 v[104:105], v14 offset:26624\n"
"v_mfma_f32_32x32x16_bf16 v[64:79], a[44:47], v[116:119], v[64:79]\n"
"ds_read_b64_tr_b16 v[106:107], v14 offset:27136\n"
"buffer_load_dwordx4 v8, s[36:39], s22 offen lds\n"
"buffer_load_dwordx4 v40, s[36:39], s22 offen offset:1024 lds\n"
"v_mfma_f32_16x16x32_bf16 v[24:27], v[20:23], a[44:47], v[24:27]\n"
"ds_read_b64_tr_b16 v[108:109], v15 offset:26624\n"
"v_mfma_f32_4x4x4_16b_bf16 v[28:31], a[32:33], a[32:33], v[28:31]\n"
"ds_read_b64_tr_b16 v[110:111], v15 offset:27136\n"
"v_mfma_f32_4x4x4_16b_bf16 v[212:215], a[34:35], a[34:35], v[212:215]\n"
"ds_read_b64_tr_b16 v[224:225], v219 offset:24576\n"
"v_mfma_f32_4x4x4_16b_bf16 v[28:31], a[36:37], a[36:37], v[28:31]\n"
"v_mfma_f32_4x4x4_16b_bf16 v[212:215], a[38:39], a[38:39], v[212:215]\n"
"ds_read_b64_tr_b16 v[226:227], v219 offset:25088\n"
"v_mfma_f32_4x4x4_16b_bf16 v[28:31], a[40:41], a[40:41], v[28:31]\n"
"v_mfma_f32_4x4x4_16b_bf16 v[212:215], a[42:43], a[42:43], v[212:215]\n"
"ds_read_b64_tr_b16 v[228:229], v220 offset:24576\n"
"v_mfma_f32_4x4x4_16b_bf16 v[28:31], a[44:45], a[44:45], v[28:31]\n"
"v_mfma_f32_4x4x4_16b_bf16 v[212:215], a[46:47], a[46:47], v[212:215]\n"
"ds_read_b64_tr_b16 v[230:231], v220 offset:25088\n"
"s_waitcnt lgkmcnt(9)\n"
"v_mfma_f32_32x32x16_bf16 v[48:63], v[96:99], a[48:51], v[48:63]\n"
"s_add_u32 m0, s67, 8192\n"
"ds_read_b128 a[56:59], v12 offset:12288\n"
"ds_read_b64_tr_b16 v[112:113], v14 offset:28672\n"
"v_mfma_f32_32x32x16_bf16 v[64:79], a[48:51], v[100:103], v[64:79]\n"
"ds_read_b64_tr_b16 v[114:115], v14 offset:29184\n"
"buffer_load_dwordx4 v4, s[28:31], s86 offen offset:0 lds\n"
"buffer_load_dwordx4 v5, s[28:31], s86 offen offset:1024 lds\n"
"buffer_load_dwordx4 v6, s[28:31], s86 offen offset:2048 lds\n"
"buffer_load_dwordx4 v7, s[28:31], s86 offen offset:3072 lds\n"
"v_mfma_f32_16x16x32_bf16 v[24:27], v[20:23], a[48:51], v[24:27]\n"
"ds_read_b64_tr_b16 v[116:117], v15 offset:28672\n"
"s_waitcnt lgkmcnt(4)\n"
"v_mfma_f32_16x16x32_bf16 v[32:35], v[20:23], v[224:227], v[32:35]\n"
"ds_read_b64_tr_b16 v[118:119], v15 offset:29184\n"
"v_mfma_f32_16x16x32_bf16 v[36:39], v[20:23], v[228:231], v[36:39]\n"
"v_mfma_f32_32x32x16_bf16 v[48:63], v[104:107], a[52:55], v[48:63]\n"
"s_add_u32 m0, s68, 57344\n"
"ds_read_b128 a[60:63], v13 offset:12288\n"
"ds_read_b64_tr_b16 v[96:97], v14 offset:30720\n"
"v_mfma_f32_32x32x16_bf16 v[64:79], a[52:55], v[108:111], v[64:79]\n"
"ds_read_b64_tr_b16 v[98:99], v14 offset:31232\n"
"buffer_load_dwordx4 v8, s[36:39], s23 offen lds\n"
"buffer_load_dwordx4 v40, s[36:39], s23 offen offset:1024 lds\n"
"v_mfma_f32_16x16x32_bf16 v[24:27], v[20:23], a[52:55], v[24:27]\n"
"ds_read_b64_tr_b16 v[100:101], v15 offset:30720\n"
"ds_read_b64_tr_b16 v[102:103], v15 offset:31232\n"
"s_waitcnt lgkmcnt(5)\n"
"v_mfma_f32_32x32x16_bf16 v[48:63], v[112:115], a[56:59], v[48:63]\n"
"s_waitcnt vmcnt(15)\n"
"s_barrier\n"
"ds_read_b128 a[64:67], v10 offset:0\n"
"ds_read_b64_tr_b16 v[104:105], v14 offset:32768\n"
"v_mfma_f32_32x32x16_bf16 v[64:79], a[56:59], v[116:119], v[64:79]\n"
"ds_read_b64_tr_b16 v[106:107], v14 offset:33280\n"
"v_mfma_f32_16x16x32_bf16 v[24:27], v[20:23], a[56:59], v[24:27]\n"
"ds_read_b64_tr_b16 v[108:109], v15 offset:32768\n"
"ds_read_b64_tr_b16 v[110:111], v15 offset:33280\n"
"s_waitcnt lgkmcnt(5)\n"
"v_mfma_f32_32x32x16_bf16 v[48:63], v[96:99], a[60:63], v[48:63]\n"
"s_add_u32 m0, s67, 12288\n"
"ds_read_b128 a[68:71], v11 offset:0\n"
"ds_read_b64_tr_b16 v[112:113], v14 offset:34816\n"
"v_mfma_f32_32x32x16_bf16 v[64:79], a[60:63], v[100:103], v[64:79]\n"
"ds_read_b64_tr_b16 v[114:115], v14 offset:35328\n"
"buffer_load_dwordx4 v4, s[28:31], s87 offen offset:0 lds\n"
"buffer_load_dwordx4 v5, s[28:31], s87 offen offset:1024 lds\n"
"v_mfma_f32_16x16x32_bf16 v[24:27], v[20:23], a[60:63], v[24:27]\n"
"ds_read_b64_tr_b16 v[116:117], v15 offset:34816\n"
"v_mfma_f32_4x4x4_16b_bf16 v[28:31], a[48:49], a[48:49], v[28:31]\n"
"ds_read_b64_tr_b16 v[118:119], v15 offset:35328\n"
"v_mfma_f32_4x4x4_16b_bf16 v[212:215], a[50:51], a[50:51], v[212:215]\n"
"ds_read_b64_tr_b16 v[224:225], v219 offset:32768\n"
"v_mfma_f32_4x4x4_16b_bf16 v[28:31], a[52:53], a[52:53], v[28:31]\n"
"v_mfma_f32_4x4x4_16b_bf16 v[212:215], a[54:55], a[54:55], v[212:215]\n"
"ds_read_b64_tr_b16 v[226:227], v219 offset:33280\n"
"v_mfma_f32_4x4x4_16b_bf16 v[28:31], a[56:57], a[56:57], v[28:31]\n"
"v_mfma_f32_4x4x4_16b_bf16 v[212:215], a[58:59], a[58:59], v[212:215]\n"
"ds_read_b64_tr_b16 v[228:229], v220 offset:32768\n"
"v_mfma_f32_4x4x4_16b_bf16 v[28:31], a[60:61], a[60:61], v[28:31]\n"
"v_mfma_f32_4x4x4_16b_bf16 v[212:215], a[62:63], a[62:63], v[212:215]\n"
"ds_read_b64_tr_b16 v[230:231], v220 offset:33280\n"
"s_waitcnt lgkmcnt(9)\n"
"v_mfma_f32_32x32x16_bf16 v[48:63], v[104:107], a[64:67], v[48:63]\n"
"ds_read_b128 a[72:75], v12 offset:0\n"
"ds_read_b64_tr_b16 v[96:97], v14 offset:36864\n"
"v_mfma_f32_32x32x16_bf16 v[64:79], a[64:67], v[108:111], v[64:79]\n"
"ds_read_b64_tr_b16 v[98:99], v14 offset:37376\n"
"buffer_load_dwordx4 v6, s[28:31], s87 offen offset:2048 lds\n"
"buffer_load_dwordx4 v7, s[28:31], s87 offen offset:3072 lds\n"
"s_add_u32 m0, s69, 69632\n"
"s_nop 0\n"
"buffer_load_dwordx4 v9, s[36:39], s89 offen lds\n"
"s_add_u32 m0, s69, 73728\n"
"s_nop 0\n"
"buffer_load_dwordx4 v9, s[36:39], s90 offen lds\n"
"v_mfma_f32_16x16x32_bf16 v[24:27], v[20:23], a[64:67], v[24:27]\n"
"ds_read_b64_tr_b16 v[100:101], v15 offset:36864\n"
"s_waitcnt lgkmcnt(4)\n"
"v_mfma_f32_16x16x32_bf16 v[32:35], v[20:23], v[224:227], v[32:35]\n"
"ds_read_b64_tr_b16 v[102:103], v15 offset:37376\n"
"v_mfma_f32_16x16x32_bf16 v[36:39], v[20:23], v[228:231], v[36:39]\n"
"v_mfma_f32_32x32x16_bf16 v[48:63], v[112:115], a[68:71], v[48:63]\n"
"s_add_u32 m0, s69, 77824\n"
"ds_read_b128 a[76:79], v13 offset:0\n"
"ds_read_b64_tr_b16 v[104:105], v14 offset:38912\n"
"v_mfma_f32_32x32x16_bf16 v[64:79], a[68:71], v[116:119], v[64:79]\n"
"ds_read_b64_tr_b16 v[106:107], v14 offset:39424\n"
"buffer_load_dwordx4 v9, s[36:39], s91 offen lds\n"
"s_add_u32 m0, s69, 86016\n"
"s_nop 0\n"
"buffer_load_dwordx4 v9, s[36:39], s93 offen lds\n"
"v_mfma_f32_16x16x32_bf16 v[24:27], v[20:23], a[68:71], v[24:27]\n"
"ds_read_b64_tr_b16 v[108:109], v15 offset:38912\n"
"ds_read_b64_tr_b16 v[110:111], v15 offset:39424\n"
"s_waitcnt lgkmcnt(5)\n"
"v_mfma_f32_32x32x16_bf16 v[48:63], v[96:99], a[72:75], v[48:63]\n"
"s_add_u32 m0, s69, 90112\n"
"s_waitcnt vmcnt(16)\n"
"s_barrier\n"
"ds_read_b128 a[80:83], v10 offset:4096\n"
"ds_read_b64_tr_b16 v[112:113], v14 offset:40960\n"
"v_mfma_f32_32x32x16_bf16 v[64:79], a[72:75], v[100:103], v[64:79]\n"
"ds_read_b64_tr_b16 v[114:115], v14 offset:41472\n"
"buffer_load_dwordx4 v9, s[36:39], s22 offen lds\n"
"s_add_u32 m0, s69, 94208\n"
"s_nop 0\n"
"buffer_load_dwordx4 v9, s[36:39], s23 offen lds\n"
"v_mfma_f32_16x16x32_bf16 v[24:27], v[20:23], a[72:75], v[24:27]\n"
"ds_read_b64_tr_b16 v[116:117], v15 offset:40960\n"
"ds_read_b64_tr_b16 v[118:119], v15 offset:41472\n"
"s_waitcnt lgkmcnt(5)\n"
"v_mfma_f32_32x32x16_bf16 v[48:63], v[104:107], a[76:79], v[48:63]\n"
"ds_read_b128 a[84:87], v11 offset:4096\n"
"ds_read_b64_tr_b16 v[96:97], v14 offset:43008\n"
"v_mfma_f32_32x32x16_bf16 v[64:79], a[76:79], v[108:111], v[64:79]\n"
"ds_read_b64_tr_b16 v[98:99], v14 offset:43520\n"
"v_mfma_f32_16x16x32_bf16 v[24:27], v[20:23], a[76:79], v[24:27]\n"
"ds_read_b64_tr_b16 v[100:101], v15 offset:43008\n"
"v_mfma_f32_4x4x4_16b_bf16 v[28:31], a[64:65], a[64:65], v[28:31]\n"
"ds_read_b64_tr_b16 v[102:103], v15 offset:43520\n"
"v_mfma_f32_4x4x4_16b_bf16 v[212:215], a[66:67], a[66:67], v[212:215]\n"
"ds_read_b64_tr_b16 v[224:225], v219 offset:40960\n"
"v_mfma_f32_4x4x4_16b_bf16 v[28:31], a[68:69], a[68:69], v[28:31]\n"
"v_mfma_f32_4x4x4_16b_bf16 v[212:215], a[70:71], a[70:71], v[212:215]\n"
"ds_read_b64_tr_b16 v[226:227], v219 offset:41472\n"
"v_mfma_f32_4x4x4_16b_bf16 v[28:31], a[72:73], a[72:73], v[28:31]\n"
"v_mfma_f32_4x4x4_16b_bf16 v[212:215], a[74:75], a[74:75], v[212:215]\n"
"ds_read_b64_tr_b16 v[228:229], v220 offset:40960\n"
"v_mfma_f32_4x4x4_16b_bf16 v[28:31], a[76:77], a[76:77], v[28:31]\n"
"v_mfma_f32_4x4x4_16b_bf16 v[212:215], a[78:79], a[78:79], v[212:215]\n"
"ds_read_b64_tr_b16 v[230:231], v220 offset:41472\n"
"s_waitcnt lgkmcnt(9)\n"
"v_mfma_f32_32x32x16_bf16 v[48:63], v[112:115], a[80:83], v[48:63]\n"
"ds_read_b128 a[88:91], v12 offset:4096\n"
"ds_read_b64_tr_b16 v[104:105], v14 offset:45056\n"
"v_mfma_f32_32x32x16_bf16 v[64:79], a[80:83], v[116:119], v[64:79]\n"
"ds_read_b64_tr_b16 v[106:107], v14 offset:45568\n"
"v_mfma_f32_16x16x32_bf16 v[24:27], v[20:23], a[80:83], v[24:27]\n"
"ds_read_b64_tr_b16 v[108:109], v15 offset:45056\n"
"s_waitcnt lgkmcnt(4)\n"
"v_mfma_f32_16x16x32_bf16 v[32:35], v[20:23], v[224:227], v[32:35]\n"
"ds_read_b64_tr_b16 v[110:111], v15 offset:45568\n"
"v_mfma_f32_16x16x32_bf16 v[36:39], v[20:23], v[228:231], v[36:39]\n"
"v_mfma_f32_32x32x16_bf16 v[48:63], v[96:99], a[84:87], v[48:63]\n"
"ds_read_b128 a[92:95], v13 offset:4096\n"
"ds_read_b64_tr_b16 v[112:113], v14 offset:47104\n"
"v_mfma_f32_32x32x16_bf16 v[64:79], a[84:87], v[100:103], v[64:79]\n"
"ds_read_b64_tr_b16 v[114:115], v14 offset:47616\n"
"v_mfma_f32_16x16x32_bf16 v[24:27], v[20:23], a[84:87], v[24:27]\n"
"ds_read_b64_tr_b16 v[116:117], v15 offset:47104\n"
"ds_read_b64_tr_b16 v[118:119], v15 offset:47616\n"
"s_waitcnt lgkmcnt(5)\n"
"v_mfma_f32_32x32x16_bf16 v[48:63], v[104:107], a[88:91], v[48:63]\n"
"s_waitcnt vmcnt(12)\n"
"s_barrier\n"
"ds_read_b128 a[96:99], v10 offset:8192\n"
"ds_read_b64_tr_b16 v[96:97], v14 offset:49152\n"
"v_mfma_f32_32x32x16_bf16 v[64:79], a[88:91], v[108:111], v[64:79]\n"
"ds_read_b64_tr_b16 v[98:99], v14 offset:49664\n"
"v_mfma_f32_16x16x32_bf16 v[24:27], v[20:23], a[88:91], v[24:27]\n"
"ds_read_b64_tr_b16 v[100:101], v15 offset:49152\n"
"ds_read_b64_tr_b16 v[102:103], v15 offset:49664\n"
"s_waitcnt lgkmcnt(5)\n"
"v_mfma_f32_32x32x16_bf16 v[48:63], v[112:115], a[92:95], v[48:63]\n"
"ds_read_b128 a[100:103], v11 offset:8192\n"
"ds_read_b64_tr_b16 v[104:105], v14 offset:51200\n"
"v_mfma_f32_32x32x16_bf16 v[64:79], a[92:95], v[116:119], v[64:79]\n"
"ds_read_b64_tr_b16 v[106:107], v14 offset:51712\n"
"v_mfma_f32_16x16x32_bf16 v[24:27], v[20:23], a[92:95], v[24:27]\n"
"ds_read_b64_tr_b16 v[108:109], v15 offset:51200\n"
"v_mfma_f32_4x4x4_16b_bf16 v[28:31], a[80:81], a[80:81], v[28:31]\n"
"ds_read_b64_tr_b16 v[110:111], v15 offset:51712\n"
"v_mfma_f32_4x4x4_16b_bf16 v[212:215], a[82:83], a[82:83], v[212:215]\n"
"ds_read_b64_tr_b16 v[224:225], v219 offset:49152\n"
"v_mfma_f32_4x4x4_16b_bf16 v[28:31], a[84:85], a[84:85], v[28:31]\n"
"v_mfma_f32_4x4x4_16b_bf16 v[212:215], a[86:87], a[86:87], v[212:215]\n"
"ds_read_b64_tr_b16 v[226:227], v219 offset:49664\n"
"v_mfma_f32_4x4x4_16b_bf16 v[28:31], a[88:89], a[88:89], v[28:31]\n"
"v_mfma_f32_4x4x4_16b_bf16 v[212:215], a[90:91], a[90:91], v[212:215]\n"
"ds_read_b64_tr_b16 v[228:229], v220 offset:49152\n"
"v_mfma_f32_4x4x4_16b_bf16 v[28:31], a[92:93], a[92:93], v[28:31]\n"
"v_mfma_f32_4x4x4_16b_bf16 v[212:215], a[94:95], a[94:95], v[212:215]\n"
"ds_read_b64_tr_b16 v[230:231], v220 offset:49664\n"
"s_waitcnt lgkmcnt(9)\n"
"v_mfma_f32_32x32x16_bf16 v[48:63], v[96:99], a[96:99], v[48:63]\n"
"ds_read_b128 a[104:107], v12 offset:8192\n"
"ds_read_b64_tr_b16 v[112:113], v14 offset:53248\n"
"v_mfma_f32_32x32x16_bf16 v[64:79], a[96:99], v[100:103], v[64:79]\n"
"ds_read_b64_tr_b16 v[114:115], v14 offset:53760\n"
"v_mfma_f32_16x16x32_bf16 v[24:27], v[20:23], a[96:99], v[24:27]\n"
"ds_read_b64_tr_b16 v[116:117], v15 offset:53248\n"
"s_waitcnt lgkmcnt(4)\n"
"v_mfma_f32_16x16x32_bf16 v[32:35], v[20:23], v[224:227], v[32:35]\n"
"ds_read_b64_tr_b16 v[118:119], v15 offset:53760\n"
"v_mfma_f32_16x16x32_bf16 v[36:39], v[20:23], v[228:231], v[36:39]\n"
"v_mfma_f32_32x32x16_bf16 v[48:63], v[104:107], a[100:103], v[48:63]\n"
"ds_read_b128 a[108:111], v13 offset:8192\n"
"ds_read_b64_tr_b16 v[96:97], v14 offset:55296\n"
"v_mfma_f32_32x32x16_bf16 v[64:79], a[100:103], v[108:111], v[64:79]\n"
"ds_read_b64_tr_b16 v[98:99], v14 offset:55808\n"
"v_mfma_f32_16x16x32_bf16 v[24:27], v[20:23], a[100:103], v[24:27]\n"
"ds_read_b64_tr_b16 v[100:101], v15 offset:55296\n"
"ds_read_b64_tr_b16 v[102:103], v15 offset:55808\n"
"s_waitcnt lgkmcnt(5)\n"
"v_mfma_f32_32x32x16_bf16 v[48:63], v[112:115], a[104:107], v[48:63]\n"
"s_waitcnt vmcnt(6)\n"
"s_barrier\n"
"ds_read_b128 a[112:115], v10 offset:12288\n"
"ds_read_b128 a[116:119], v11 offset:12288\n"
"v_mfma_f32_32x32x16_bf16 v[64:79], a[104:107], v[116:119], v[64:79]\n"
"ds_read_b128 a[120:123], v12 offset:12288\n"
"ds_read_b128 a[124:127], v13 offset:12288\n"
"ds_read_b64_tr_b16 v[224:225], v219 offset:57344\n"
"ds_read_b64_tr_b16 v[226:227], v219 offset:57856\n"
"v_mfma_f32_16x16x32_bf16 v[24:27], v[20:23], a[104:107], v[24:27]\n"
"ds_read_b64_tr_b16 v[228:229], v220 offset:57344\n"
"v_mfma_f32_4x4x4_16b_bf16 v[28:31], a[96:97], a[96:97], v[28:31]\n"
"ds_read_b64_tr_b16 v[230:231], v220 offset:57856\n"
"v_mfma_f32_4x4x4_16b_bf16 v[212:215], a[98:99], a[98:99], v[212:215]\n"
"v_mfma_f32_4x4x4_16b_bf16 v[28:31], a[100:101], a[100:101], v[28:31]\n"
"ds_read_b64_tr_b16 v[104:105], v14 offset:57344\n"
"v_mfma_f32_4x4x4_16b_bf16 v[212:215], a[102:103], a[102:103], v[212:215]\n"
"v_mfma_f32_4x4x4_16b_bf16 v[28:31], a[104:105], a[104:105], v[28:31]\n"
"ds_read_b64_tr_b16 v[106:107], v14 offset:57856\n"
"v_mfma_f32_4x4x4_16b_bf16 v[212:215], a[106:107], a[106:107], v[212:215]\n"
"s_waitcnt lgkmcnt(10)\n"
"v_mfma_f32_32x32x16_bf16 v[48:63], v[96:99], a[108:111], v[48:63]\n"
"ds_read_b64_tr_b16 v[112:113], v14 offset:59392\n"
"ds_read_b64_tr_b16 v[114:115], v14 offset:59904\n"
"ds_read_b64_tr_b16 a[148:149], v14 offset:63488\n"
"v_mfma_f32_32x32x16_bf16 v[64:79], a[108:111], v[100:103], v[64:79]\n"
"ds_read_b64_tr_b16 a[150:151], v14 offset:64000\n"
"ds_read_b64_tr_b16 v[108:109], v15 offset:57344\n"
"ds_read_b64_tr_b16 v[110:111], v15 offset:57856\n"
"s_waitcnt lgkmcnt(8)\n"
"v_mfma_f32_16x16x32_bf16 v[32:35], v[20:23], v[224:227], v[32:35]\n"
"ds_read_b64_tr_b16 v[96:97], v14 offset:61440\n"
"v_mfma_f32_16x16x32_bf16 v[36:39], v[20:23], v[228:231], v[36:39]\n"
"ds_read_b64_tr_b16 v[98:99], v14 offset:61952\n"
"v_mfma_f32_4x4x4_16b_bf16 v[28:31], a[108:109], a[108:109], v[28:31]\n"
"v_mfma_f32_4x4x4_16b_bf16 v[212:215], a[110:111], a[110:111], v[212:215]\n"
"ds_read_b64_tr_b16 v[116:117], v15 offset:59392\n"
"v_mfma_f32_4x4x4_16b_bf16 v[28:31], a[112:113], a[112:113], v[28:31]\n"
"v_mfma_f32_4x4x4_16b_bf16 v[212:215], a[114:115], a[114:115], v[212:215]\n"
"ds_read_b64_tr_b16 v[118:119], v15 offset:59904\n"
"v_mfma_f32_4x4x4_16b_bf16 v[28:31], a[116:117], a[116:117], v[28:31]\n"
"v_mfma_f32_4x4x4_16b_bf16 v[212:215], a[118:119], a[118:119], v[212:215]\n"
"ds_write2_b32 v248, v32, v36 offset0:0 offset1:1\n"
"v_mfma_f32_4x4x4_16b_bf16 v[28:31], a[120:121], a[120:121], v[28:31]\n"
"v_mfma_f32_4x4x4_16b_bf16 v[212:215], a[122:123], a[122:123], v[212:215]\n"
"ds_read_b64_tr_b16 v[100:101], v15 offset:61440\n"
"v_mfma_f32_4x4x4_16b_bf16 v[28:31], a[124:125], a[124:125], v[28:31]\n"
"v_mfma_f32_4x4x4_16b_bf16 v[212:215], a[126:127], a[126:127], v[212:215]\n"
"ds_read_b64_tr_b16 v[102:103], v15 offset:61952\n"
"v_mfma_f32_16x16x32_bf16 v[24:27], v[20:23], a[108:111], v[24:27]\n"
"s_waitcnt vmcnt(0) lgkmcnt(2)\n"
"s_barrier\n"
"v_mfma_f32_16x16x32_bf16 v[24:27], v[20:23], a[112:115], v[24:27]\n"
"ds_read_b64 v[224:225], v249 offset:0\n"
"ds_read_b64 v[226:227], v249 offset:16384\n"
"v_mfma_f32_16x16x32_bf16 v[24:27], v[20:23], a[116:119], v[24:27]\n"
"ds_read_b64 v[228:229], v249 offset:32768\n"
"ds_read_b64 v[230:231], v249 offset:49152\n"
"v_mfma_f32_16x16x32_bf16 v[24:27], v[20:23], a[120:123], v[24:27]\n"
"v_add_f32 v28, v28, v212\n"
"v_add_f32 v29, v29, v213\n"
"v_mfma_f32_16x16x32_bf16 v[24:27], v[20:23], a[124:127], v[24:27]\n"
"v_add_f32 v30, v30, v214\n"
"v_add_f32 v31, v31, v215\n"
"v_mfma_f32_32x32x16_bf16 v[48:63], v[104:107], a[112:115], v[48:63]\n"
"ds_read_b64_tr_b16 a[152:153], v15 offset:63488\n"
"v_cndmask_b32 v28, v28, v29, s[24:25]\n"
"v_cndmask_b32 v28, v28, v30, s[26:27]\n"
"v_cndmask_b32 v28, v28, v31, s[8:9]\n"
"ds_read_b64_tr_b16 a[154:155], v15 offset:64000\n"
"v_mov_b32 v29, v28\n"
"v_mfma_f32_32x32x16_bf16 v[48:63], v[112:115], a[116:119], v[48:63]\n"
"v_mul_f32 v24, 0xbb000000, v24\n"
"v_permlane32_swap_b32 v28, v29\n"
"v_mul_f32 v30, v24, v24\n"
"v_add_f32 v28, v28, v29\n"
"v_cvt_pk_bf16_f32 v25, v24, v24\n"
"v_fma_f32 v28, v28, v216, -v30\n"
"v_mfma_f32_32x32x16_bf16 v[48:63], a[148:151], a[124:127], v[48:63]\n"
"s_waitcnt lgkmcnt(2)\n"
"v_add_f32 v224, v224, v226\n"
"v_max_f32 v28, 0, v28\n"
"v_add_f32 v228, v228, v230\n"
"v_add_f32 v28, 0x3727c5ac, v28\n"
"v_add_f32 v224, v224, v228\n"
"v_mfma_f32_32x32x16_bf16 v[48:63], v[96:99], a[120:123], v[48:63]\n"
"v_rsq_f32 v28, v28\n"
"v_lshlrev_b32 v26, 16, v25\n"
"v_cvt_pk_bf16_f32 v226, v224, v224\n"
"v_sub_f32 v26, v24, v26\n"
"v_lshlrev_b32 v226, 16, v226\n"
"v_mfma_f32_32x32x16_bf16 v[64:79], a[112:115], v[108:111], v[64:79]\n"
"v_cvt_pk_bf16_f32 v26, v26, v26\n"
"v_sub_f32 v226, v224, v226\n"
"v_cndmask_b32 v128, v26, v25, s[10:11]\n"
"v_cvt_pk_bf16_f32 v132, v224, v226\n"
"v_cvt_pk_bf16_f32 v140, v28, v28\n"
"v_add_f32 v225, v225, v227\n"
"v_mfma_f32_32x32x16_bf16 v[48:63], v[132:135], v[128:131], v[48:63]\n"
"v_lshlrev_b32 v140, 16, v140\n"
"v_add_f32 v229, v229, v231\n"
"v_sub_f32 v140, v28, v140\n"
"v_add_f32 v225, v225, v229\n"
"v_cvt_pk_bf16_f32 v140, v28, v140\n"
"v_cvt_pk_bf16_f32 v227, v225, v225\n"
"v_mfma_f32_32x32x16_bf16 v[64:79], a[116:119], v[116:119], v[64:79]\n"
"v_cndmask_b32 v140, 0, v140, s[10:11]\n"
"v_lshlrev_b32 v227, 16, v227\n"
"ds_read_b64_tr_b16 a[132:133], v16 offset:0\n"
"ds_read_b64_tr_b16 a[134:135], v16 offset:256\n"
"v_sub_f32 v227, v225, v227\n"
"v_cvt_pk_bf16_f32 v136, v225, v227\n"
"v_mfma_f32_32x32x16_bf16 v[80:95], v[140:143], v[208:211], 0\n"
"v_mul_f32 v48, v48, v28\n"
"v_mul_f32 v49, v49, v28\n"
"v_mul_f32 v50, v50, v28\n"
"v_mul_f32 v51, v51, v28\n"
"v_mul_f32 v52, v52, v28\n"
"v_mul_f32 v53, v53, v28\n"
"v_mul_f32 v54, v54, v28\n"
"v_mfma_f32_32x32x16_bf16 v[64:79], a[120:123], v[100:103], v[64:79]\n"
"v_mul_f32 v55, v55, v28\n"
"v_cvt_pk_bf16_f32 v48, v48, v49\n"
"v_cvt_pk_bf16_f32 v49, v50, v51\n"
"v_cvt_pk_bf16_f32 v50, v52, v53\n"
"v_cvt_pk_bf16_f32 v51, v54, v55\n"
"buffer_store_dwordx4 v[48:51], v217, s[40:43], s64 offen sc1\n"
"v_mul_f32 v56, v56, v28\n"
"v_mfma_f32_32x32x16_bf16 v[64:79], v[128:131], v[136:139], v[64:79]\n"
"v_mul_f32 v57, v57, v28\n"
"v_mul_f32 v58, v58, v28\n"
"v_mul_f32 v59, v59, v28\n"
"v_mul_f32 v60, v60, v28\n"
"v_mul_f32 v61, v61, v28\n"
"v_mul_f32 v62, v62, v28\n"
"ds_read_b64_tr_b16 a[136:137], v16 offset:1024\n"
"ds_read_b64_tr_b16 a[138:139], v16 offset:1280\n"
"s_waitcnt lgkmcnt(4)\n"
"v_mfma_f32_32x32x16_bf16 v[64:79], a[124:127], a[152:155], v[64:79]\n"
"v_mul_f32 v63, v63, v28\n"
"v_cvt_pk_bf16_f32 v56, v56, v57\n"
"v_cvt_pk_bf16_f32 v57, v58, v59\n"
"v_cvt_pk_bf16_f32 v58, v60, v61\n"
"v_cvt_pk_bf16_f32 v59, v62, v63\n"
"buffer_store_dwordx4 v[56:59], v217, s[40:43], s65 offen sc1\n"
"ds_read_b64_tr_b16 a[140:141], v16 offset:2048\n"
"ds_read_b64_tr_b16 a[142:143], v16 offset:2304\n"
"s_waitcnt lgkmcnt(4)\n"
"v_mfma_f32_32x32x16_bf16 v[192:207], a[132:135], a[0:3], 0\n"
"ds_read_b64_tr_b16 a[144:145], v16 offset:3072\n"
"ds_read_b64_tr_b16 a[146:147], v16 offset:3328\n"
"ds_write_b128 v122, v[48:51] offset:0\n"
"s_waitcnt lgkmcnt(5)\n"
"v_mfma_f32_32x32x16_bf16 v[192:207], a[136:139], a[4:7], v[192:207]\n"
"ds_read_b64_tr_b16 a[132:133], v16 offset:4096\n"
"ds_read_b64_tr_b16 a[134:135], v16 offset:4352\n"
"v_pk_mul_f32 v[64:65], v[64:65], v[80:81]\n"
"v_pk_mul_f32 v[66:67], v[66:67], v[82:83]\n"
"v_pk_mul_f32 v[68:69], v[68:69], v[84:85]\n"
"v_pk_mul_f32 v[70:71], v[70:71], v[86:87]\n"
"v_cvt_pk_bf16_f32 v64, v64, v65\n"
"v_cvt_pk_bf16_f32 v65, v66, v67\n"
"v_cvt_pk_bf16_f32 v66, v68, v69\n"
"v_cvt_pk_bf16_f32 v67, v70, v71\n"
"buffer_store_dwordx4 v[64:67], v218, s[40:43], s64 offen offset:1024 sc1\n"
"v_pk_mul_f32 v[72:73], v[72:73], v[88:89]\n"
"v_pk_mul_f32 v[74:75], v[74:75], v[90:91]\n"
"v_pk_mul_f32 v[76:77], v[76:77], v[92:93]\n"
"v_pk_mul_f32 v[78:79], v[78:79], v[94:95]\n"
"v_cvt_pk_bf16_f32 v72, v72, v73\n"
"v_cvt_pk_bf16_f32 v73, v74, v75\n"
"v_cvt_pk_bf16_f32 v74, v76, v77\n"
"v_cvt_pk_bf16_f32 v75, v78, v79\n"
"buffer_store_dwordx4 v[72:75], v218, s[40:43], s64 offen offset:1536 sc1\n"
"s_waitcnt lgkmcnt(5)\n"
"v_mfma_f32_32x32x16_bf16 v[192:207], a[140:143], a[8:11], v[192:207]\n"
"ds_read_b64_tr_b16 a[136:137], v16 offset:5120\n"
"ds_read_b64_tr_b16 a[138:139], v16 offset:5376\n"
"ds_write_b128 v122, v[56:59] offset:8192\n"
"s_waitcnt lgkmcnt(6)\n"
"v_mfma_f32_32x32x16_bf16 v[192:207], a[144:147], a[12:15], v[192:207]\n"
"ds_read_b64_tr_b16 a[140:141], v16 offset:6144\n"
"ds_read_b64_tr_b16 a[142:143], v16 offset:6400\n"
"ds_write_b128 v123, v[64:67] offset:0\n"
"s_waitcnt lgkmcnt(6)\n"
"v_mfma_f32_32x32x16_bf16 v[192:207], a[132:135], a[16:19], v[192:207]\n"
"ds_read_b64_tr_b16 a[144:145], v16 offset:7168\n"
"ds_read_b64_tr_b16 a[146:147], v16 offset:7424\n"
"ds_write_b128 v123, v[72:75] offset:512\n"
"ds_read_b64_tr_b16 v[224:225], v250 offset:0\n"
"ds_read_b64_tr_b16 v[226:227], v250 offset:256\n"
"s_waitcnt lgkmcnt(9)\n"
"v_mfma_f32_32x32x16_bf16 v[192:207], a[136:139], a[20:23], v[192:207]\n"
"ds_read_b64_tr_b16 a[132:133], v16 offset:8192\n"
"ds_read_b64_tr_b16 a[134:135], v16 offset:8448\n"
"ds_read_b64_tr_b16 v[228:229], v250 offset:4096\n"
"ds_read_b64_tr_b16 v[230:231], v250 offset:4352\n"
"s_waitcnt lgkmcnt(10)\n"
"v_mfma_f32_32x32x16_bf16 v[192:207], a[140:143], a[24:27], v[192:207]\n"
"ds_read_b64_tr_b16 a[136:137], v16 offset:9216\n"
"ds_read_b64_tr_b16 a[138:139], v16 offset:9472\n"
"ds_read_b64_tr_b16 v[232:233], v250 offset:8192\n"
"ds_read_b64_tr_b16 v[234:235], v250 offset:8448\n"
"s_waitcnt lgkmcnt(8)\n"
"v_mfma_f32_16x16x32_bf16 v[32:35], v[20:23], v[224:227], 0\n"
"v_mfma_f32_32x32x16_bf16 v[192:207], a[144:147], a[28:31], v[192:207]\n"
"ds_read_b64_tr_b16 a[140:141], v16 offset:10240\n"
"ds_read_b64_tr_b16 a[142:143], v16 offset:10496\n"
"ds_read_b64_tr_b16 v[236:237], v250 offset:12288\n"
"ds_read_b64_tr_b16 v[238:239], v250 offset:12544\n"
"s_waitcnt lgkmcnt(8)\n"
"v_mfma_f32_16x16x32_bf16 v[32:35], v[20:23], v[228:231], v[32:35]\n"
"v_mfma_f32_32x32x16_bf16 v[192:207], a[132:135], a[32:35], v[192:207]\n"
"ds_read_b64_tr_b16 a[144:145], v16 offset:11264\n"
"ds_read_b64_tr_b16 a[146:147], v16 offset:11520\n"
"ds_read_b64_tr_b16 v[224:225], v250 offset:16384\n"
"ds_read_b64_tr_b16 v[226:227], v250 offset:16640\n"
"s_waitcnt lgkmcnt(8)\n"
"v_mfma_f32_16x16x32_bf16 v[32:35], v[20:23], v[232:235], v[32:35]\n"
"v_mfma_f32_32x32x16_bf16 v[192:207], a[136:139], a[36:39], v[192:207]\n"
"ds_read_b64_tr_b16 a[132:133], v16 offset:12288\n"
"ds_read_b64_tr_b16 a[134:135], v16 offset:12544\n"
"ds_read_b64_tr_b16 v[228:229], v250 offset:20480\n"
"ds_read_b64_tr_b16 v[230:231], v250 offset:20736\n"
"s_waitcnt lgkmcnt(8)\n"
"v_mfma_f32_16x16x32_bf16 v[32:35], v[20:23], v[236:239], v[32:35]\n"
"v_mfma_f32_32x32x16_bf16 v[192:207], a[140:143], a[40:43], v[192:207]\n"
"ds_read_b64_tr_b16 a[136:137], v16 offset:13312\n"
"ds_read_b64_tr_b16 a[138:139], v16 offset:13568\n"
"ds_read_b64_tr_b16 v[232:233], v250 offset:24576\n"
"ds_read_b64_tr_b16 v[234:235], v250 offset:24832\n"
"s_waitcnt lgkmcnt(8)\n"
"v_mfma_f32_16x16x32_bf16 v[32:35], v[20:23], v[224:227], v[32:35]\n"
"v_mfma_f32_32x32x16_bf16 v[192:207], a[144:147], a[44:47], v[192:207]\n"
"ds_read_b64_tr_b16 a[140:141], v16 offset:14336\n"
"ds_read_b64_tr_b16 a[142:143], v16 offset:14592\n"
"ds_read_b64_tr_b16 v[236:237], v250 offset:28672\n"
"ds_read_b64_tr_b16 v[238:239], v250 offset:28928\n"
"s_waitcnt lgkmcnt(8)\n"
"v_mfma_f32_16x16x32_bf16 v[32:35], v[20:23], v[228:231], v[32:35]\n"
"v_mfma_f32_32x32x16_bf16 v[192:207], a[132:135], a[48:51], v[192:207]\n"
"ds_read_b64_tr_b16 a[144:145], v16 offset:15360\n"
"ds_read_b64_tr_b16 a[146:147], v16 offset:15616\n"
"s_waitcnt lgkmcnt(6)\n"
"v_mfma_f32_16x16x32_bf16 v[32:35], v[20:23], v[232:235], v[32:35]\n"
"v_mfma_f32_32x32x16_bf16 v[192:207], a[136:139], a[52:55], v[192:207]\n"
"ds_read_b64_tr_b16 a[132:133], v16 offset:16384\n"
"ds_read_b64_tr_b16 a[134:135], v16 offset:16640\n"
"s_waitcnt lgkmcnt(4)\n"
"v_mfma_f32_16x16x32_bf16 v[32:35], v[20:23], v[236:239], v[32:35]\n"
"v_mfma_f32_32x32x16_bf16 v[192:207], a[140:143], a[56:59], v[192:207]\n"
"ds_read_b64_tr_b16 a[136:137], v16 offset:17408\n"
"ds_read_b64_tr_b16 a[138:139], v16 offset:17664\n"
"v_mov_b32 v40, 0\n"
"v_mul_f32 v28, 0x3eb8aa3b, v28\n"
"s_mov_b32 s68, 0\n"
"s_waitcnt lgkmcnt(4)\n"
"v_mfma_f32_32x32x16_bf16 v[192:207], a[144:147], a[60:63], v[192:207]\n"
"ds_read_b64_tr_b16 a[140:141], v16 offset:18432\n"
"ds_read_b64_tr_b16 a[142:143], v16 offset:18688\n"
"v_mov_b64 v[116:117], 0\n"
"v_mov_b64 v[118:119], 0\n"
"v_mov_b32 v7, 1\n"
"v_lshlrev_b32 v5, 25, v1\n"
"s_waitcnt lgkmcnt(4)\n"
"v_mfma_f32_32x32x16_bf16 v[192:207], a[132:135], a[64:67], v[192:207]\n"
"ds_read_b64_tr_b16 a[144:145], v16 offset:19456\n"
"ds_read_b64_tr_b16 a[146:147], v16 offset:19712\n"
"ds_write_b32 v248, v32 offset:2048\n"
"v_lshl_or_b32 v4, s56, 25, v5\n"
"s_waitcnt lgkmcnt(0)\n"
"s_waitcnt vmcnt(0)\n"
"s_barrier\n"
"v_mfma_f32_32x32x16_bf16 v[192:207], a[136:139], a[68:71], v[192:207]\n"
"ds_read_b32 v224, v249 offset:2048\n"
"ds_read_b32 v225, v249 offset:18432\n"
"ds_read_b32 v226, v249 offset:34816\n"
"ds_read_b32 v227, v249 offset:51200\n"
"ds_read_b64_tr_b16 a[132:133], v16 offset:20480\n"
"ds_read_b64_tr_b16 a[134:135], v16 offset:20736\n"
"s_cmp_eq_u32 s56, 1\n"
"s_cselect_b64 exec, 1, 0\n"
"buffer_store_dwordx2 v[18:19], off, s[40:43], s61 offset:0 sc1\n"
"buffer_store_dwordx4 v[116:119], off, s[40:43], s61 offset:256 sc1\n"
"buffer_store_dwordx4 v[116:119], off, s[40:43], s61 offset:384 sc1\n"
"s_mov_b64 exec, -1\n"
"v_mfma_f32_32x32x16_bf16 v[192:207], a[140:143], a[72:75], v[192:207]\n"
"ds_read_b64_tr_b16 a[136:137], v16 offset:21504\n"
"ds_read_b64_tr_b16 a[138:139], v16 offset:21760\n"
"s_mov_b64 exec, s[76:77]\n"
"ds_read_b128 a[164:167], v222 offset:0\n"
"ds_read_b128 a[168:171], v222 offset:512\n"
"ds_read_b128 a[176:179], v222 offset:2048\n"
"ds_read_b128 a[180:183], v222 offset:2560\n"
"s_mov_b64 exec, -1\n"
"L_pad4:\n"
"s_cbranch_execz L_ret0\n"
"v_mfma_f32_32x32x16_bf16 v[192:207], a[144:147], a[76:79], v[192:207]\n"
"ds_read_b64_tr_b16 a[140:141], v16 offset:22528\n"
"ds_read_b64_tr_b16 a[142:143], v16 offset:22784\n"
"ds_read_b128 a[160:163], v125 offset:0\n"
"ds_read_b128 a[172:175], v125 offset:2048\n"
"s_waitcnt lgkmcnt(10)\n"
"v_mfma_f32_32x32x16_bf16 v[192:207], a[132:135], a[80:83], v[192:207]\n"
"ds_read_b64_tr_b16 a[144:145], v16 offset:23552\n"
"ds_read_b64_tr_b16 a[146:147], v16 offset:23808\n"
"s_waitcnt lgkmcnt(10)\n"
"v_mfma_f32_32x32x16_bf16 v[192:207], a[136:139], a[84:87], v[192:207]\n"
"ds_read_b64_tr_b16 a[132:133], v16 offset:24576\n"
"ds_read_b64_tr_b16 a[134:135], v16 offset:24832\n"
"s_waitcnt lgkmcnt(6)\n"
"v_mfma_f32_32x32x16_bf16 v[192:207], a[140:143], a[88:91], v[192:207]\n"
"ds_read_b64_tr_b16 a[136:137], v16 offset:25600\n"
"ds_read_b64_tr_b16 a[138:139], v16 offset:25856\n"
"v_add_f32 v224, v224, v225\n"
"v_add_f32 v226, v226, v227\n"
"v_add_f32 v32, v224, v226\n"
"s_waitcnt lgkmcnt(4)\n"
"v_mfma_f32_32x32x16_bf16 v[192:207], a[144:147], a[92:95], v[192:207]\n"
"ds_read_b64_tr_b16 a[140:141], v16 offset:26624\n"
"ds_read_b64_tr_b16 a[142:143], v16 offset:26880\n"
"v_cvt_pk_bf16_f32 v33, v32, v32\n"
"v_lshlrev_b32 v33, 16, v33\n"
"v_sub_f32 v33, v32, v33\n"
"s_waitcnt lgkmcnt(4)\n"
"v_mfma_f32_32x32x16_bf16 v[192:207], a[132:135], a[96:99], v[192:207]\n"
"ds_read_b64_tr_b16 a[144:145], v16 offset:27648\n"
"ds_read_b64_tr_b16 a[146:147], v16 offset:27904\n"
"v_cvt_pk_bf16_f32 v132, v32, v33\n"
"s_waitcnt lgkmcnt(4)\n"
"v_mfma_f32_32x32x16_bf16 v[192:207], a[136:139], a[100:103], v[192:207]\n"
"ds_read_b64_tr_b16 a[132:133], v16 offset:28672\n"
"ds_read_b64_tr_b16 a[134:135], v16 offset:28928\n"
"s_waitcnt lgkmcnt(4)\n"
"v_mfma_f32_32x32x16_bf16 v[192:207], a[140:143], a[104:107], v[192:207]\n"
"ds_read_b64_tr_b16 a[136:137], v16 offset:29696\n"
"ds_read_b64_tr_b16 a[138:139], v16 offset:29952\n"
"v_mfma_f32_32x32x16_bf16 v[192:207], v[132:135], v[128:131], v[192:207]\n"
"s_waitcnt lgkmcnt(4)\n"
"v_mfma_f32_32x32x16_bf16 v[192:207], a[144:147], a[108:111], v[192:207]\n"
"ds_read_b64_tr_b16 a[140:141], v16 offset:30720\n"
"ds_read_b64_tr_b16 a[142:143], v16 offset:30976\n"
"s_waitcnt lgkmcnt(4)\n"
"v_mfma_f32_32x32x16_bf16 v[192:207], a[132:135], a[112:115], v[192:207]\n"
"ds_read_b64_tr_b16 a[144:145], v16 offset:31744\n"
"ds_read_b64_tr_b16 a[146:147], v16 offset:32000\n"
"v_accvgpr_mov_b32 a152, a255\n"
"v_accvgpr_mov_b32 a153, a255\n"
"v_accvgpr_mov_b32 a154, a255\n"
"s_waitcnt lgkmcnt(4)\n"
"v_mfma_f32_32x32x16_bf16 v[192:207], a[136:139], a[116:119], v[192:207]\n"
"v_accvgpr_mov_b32 a155, a255\n"
"v_accvgpr_mov_b32 a156, a255\n"
"v_accvgpr_mov_b32 a157, a255\n"
"s_waitcnt lgkmcnt(2)\n"
"v_mfma_f32_32x32x16_bf16 v[192:207], a[140:143], a[120:123], v[192:207]\n"
"v_accvgpr_mov_b32 a158, a255\n"
"v_accvgpr_mov_b32 a159, a255\n"
"s_waitcnt lgkmcnt(0)\n"
"v_mfma_f32_32x32x16_bf16 v[192:207], a[144:147], a[124:127], v[192:207]\n"
"s_xor_b32 s0, s0, 8192\n"
"s_nop 10\n"
"v_pk_mul_f32 v[192:193], v[192:193], v[28:29] op_sel_hi:[1,0]\n"
"v_pk_mul_f32 v[194:195], v[194:195], v[28:29] op_sel_hi:[1,0]\n"
"v_pk_mul_f32 v[196:197], v[196:197], v[28:29] op_sel_hi:[1,0]\n"
"v_pk_mul_f32 v[198:199], v[198:199], v[28:29] op_sel_hi:[1,0]\n"
"v_cvt_pk_bf16_f32 v208, v192, v193\n"
"v_cvt_pk_bf16_f32 v209, v194, v195\n"
"v_cvt_pk_bf16_f32 v210, v196, v197\n"
"v_cvt_pk_bf16_f32 v211, v198, v199\n"
"ds_write_b128 v251, v[208:211] offset:0\n"
"v_pk_mul_f32 v[200:201], v[200:201], v[28:29] op_sel_hi:[1,0]\n"
"v_pk_mul_f32 v[202:203], v[202:203], v[28:29] op_sel_hi:[1,0]\n"
"v_pk_mul_f32 v[204:205], v[204:205], v[28:29] op_sel_hi:[1,0]\n"
"v_pk_mul_f32 v[206:207], v[206:207], v[28:29] op_sel_hi:[1,0]\n"
"v_cvt_pk_bf16_f32 v212, v200, v201\n"
"v_cvt_pk_bf16_f32 v213, v202, v203\n"
"v_cvt_pk_bf16_f32 v214, v204, v205\n"
"v_cvt_pk_bf16_f32 v215, v206, v207\n"
"ds_write_b128 v251, v[212:215] offset:4096\n"
"v_mfma_f32_32x32x16_bf16 v[192:207], v[116:119], v[116:119], 0\n"
"s_waitcnt lgkmcnt(0)\n"
"s_barrier\n"
"ds_read_b128 v[128:131], v252 offset:0\n"
"ds_read_b128 v[132:135], v252 offset:1024\n"
"ds_read_b128 v[136:139], v253 offset:0\n"
"ds_read_b128 v[140:143], v253 offset:1024\n"
"s_waitcnt lgkmcnt(3)\n"
"v_mfma_f32_32x32x16_bf16 v[208:223], a[160:163], v[128:131], 0\n"
"s_nop 11\n"
"v_exp_f32 v208, v208\n"
"v_exp_f32 v209, v209\n"
"v_exp_f32 v210, v210\n"
"v_exp_f32 v211, v211\n"
"v_exp_f32 v212, v212\n"
"s_waitcnt lgkmcnt(2)\n"
"v_mfma_f32_32x32x16_bf16 v[224:239], a[160:163], v[132:135], 0\n"
"v_exp_f32 v213, v213\n"
"v_exp_f32 v214, v214\n"
"v_exp_f32 v215, v215\n"
"v_exp_f32 v216, v216\n"
"v_exp_f32 v217, v217\n"
"v_smfmac_f32_32x32x32_bf16 v[144:159], a[164:167], v[208:215], v45\n"
"buffer_load_dwordx2 v[8:9], v44, s[40:43], 0 offen sc1\n"
"v_exp_f32 v218, v218\n"
"v_exp_f32 v219, v219\n"
"v_exp_f32 v220, v220\n"
"v_exp_f32 v221, v221\n"
"v_exp_f32 v222, v222\n"
"v_exp_f32 v223, v223\n"
"v_exp_f32 v224, v224\n"
"v_exp_f32 v225, v225\n"
"v_smfmac_f32_32x32x32_bf16 v[144:159], a[168:171], v[216:223], v45\n"
"v_exp_f32 v226, v226\n"
"v_exp_f32 v227, v227\n"
"v_exp_f32 v228, v228\n"
"s_waitcnt lgkmcnt(1)\n"
"v_mfma_f32_32x32x16_bf16 v[208:223], a[160:163], v[136:139], 0\n"
"v_exp_f32 v229, v229\n"
"v_exp_f32 v230, v230\n"
"v_exp_f32 v231, v231\n"
"v_exp_f32 v232, v232\n"
"v_exp_f32 v233, v233\n"
"v_smfmac_f32_32x32x32_bf16 v[160:175], a[164:167], v[224:231], v45\n"
"v_exp_f32 v234, v234\n"
"v_exp_f32 v235, v235\n"
"v_exp_f32 v236, v236\n"
"v_exp_f32 v237, v237\n"
"v_exp_f32 v238, v238\n"
"v_exp_f32 v239, v239\n"
"v_exp_f32 v208, v208\n"
"v_exp_f32 v209, v209\n"
"v_smfmac_f32_32x32x32_bf16 v[160:175], a[168:171], v[232:239], v45\n"
"v_exp_f32 v210, v210\n"
"v_exp_f32 v211, v211\n"
"v_exp_f32 v212, v212\n"
"s_waitcnt lgkmcnt(0)\n"
"v_mfma_f32_32x32x16_bf16 v[224:239], a[160:163], v[140:143], 0\n"
"v_exp_f32 v213, v213\n"
"v_exp_f32 v214, v214\n"
"v_exp_f32 v215, v215\n"
"v_exp_f32 v216, v216\n"
"v_exp_f32 v217, v217\n"
"v_smfmac_f32_32x32x32_bf16 v[176:191], a[164:167], v[208:215], v45\n"
"v_exp_f32 v218, v218\n"
"v_exp_f32 v219, v219\n"
"v_exp_f32 v220, v220\n"
"v_exp_f32 v221, v221\n"
"v_exp_f32 v222, v222\n"
"v_exp_f32 v223, v223\n"
"v_exp_f32 v224, v224\n"
"v_exp_f32 v225, v225\n"
"v_smfmac_f32_32x32x32_bf16 v[176:191], a[168:171], v[216:223], v45\n"
"v_exp_f32 v226, v226\n"
"v_exp_f32 v227, v227\n"
"v_exp_f32 v228, v228\n"
"v_mfma_f32_32x32x16_bf16 v[208:223], a[172:175], v[128:131], 0\n"
"v_exp_f32 v229, v229\n"
"v_exp_f32 v230, v230\n"
"v_exp_f32 v231, v231\n"
"v_exp_f32 v232, v232\n"
"v_exp_f32 v233, v233\n"
"v_smfmac_f32_32x32x32_bf16 v[192:207], a[164:167], v[224:231], v45\n"
"ds_write_b32 v40, v40 offset:41984\n"
"v_exp_f32 v234, v234\n"
"v_exp_f32 v235, v235\n"
"v_exp_f32 v236, v236\n"
"v_exp_f32 v237, v237\n"
"v_exp_f32 v238, v238\n"
"v_exp_f32 v239, v239\n"
"v_exp_f32 v208, v208\n"
"v_exp_f32 v209, v209\n"
"v_smfmac_f32_32x32x32_bf16 v[192:207], a[168:171], v[232:239], v45\n"
"s_mov_b64 exec, 0\n"
"s_cmp_eq_u32 s56, 0\n"
"s_cbranch_scc1 L_pad5\n"
"s_cmp_eq_u32 s56, 1\n"
"s_cbranch_scc1 L_pad6\n"
"s_cmp_eq_u32 s56, 2\n"
"s_cbranch_scc1 L_pad7\n"
"s_cmp_eq_u32 s56, 3\n"
"s_cbranch_scc1 L_padp\n"
"L_ret10:\n"
"s_mov_b64 exec, -1\n"
"s_lshl2_add_u32 s60, s54, s56\n"
"s_lshl_b32 s60, s60, 10\n"
"v_and_b32 v250, 7, v1\n"
"v_lshl_add_u32 v250, v250, 7, s60\n"
"s_xor_b32 s80, s35, 0\n"
"s_xor_b32 s81, s35, 8192\n"
"s_xor_b32 s82, s35, 16384\n"
"s_xor_b32 s83, s35, 24576\n"
"s_lshl_b32 s97, s55, 16\n"
"s_add_u32 s97, s97, s70\n"
"s_waitcnt vmcnt(0)\n"
"v_cmp_ne_u64 vcc, s[58:59], v[8:9]\n"
"s_cbranch_vccnz L_kslow\n"
"L_kback:\n"
"v_exp_f32 v210, v210\n"
"v_exp_f32 v211, v211\n"
"v_exp_f32 v212, v212\n"
"v_mfma_f32_32x32x16_bf16 v[224:239], a[172:175], v[132:135], 0\n"
"s_mov_b64 exec, s[76:77]\n"
"buffer_load_dwordx4 a[188:191], v47, s[40:43], s0 offen offset:1024 sc1\n"
"buffer_load_dwordx4 a[192:195], v47, s[40:43], s0 offen offset:1536 sc1\n"
"s_mov_b64 exec, -1\n"
"buffer_load_dwordx4 a[184:187], v2, s[40:43], s0 offen offset:0 sc1\n"
"v_exp_f32 v213, v213\n"
"v_exp_f32 v214, v214\n"
"v_exp_f32 v215, v215\n"
"v_exp_f32 v216, v216\n"
"v_exp_f32 v217, v217\n"
"v_smfmac_f32_32x32x32_bf16 v[144:159], a[176:179], v[208:215], v45\n"
"v_exp_f32 v218, v218\n"
"v_exp_f32 v219, v219\n"
"v_exp_f32 v220, v220\n"
"v_exp_f32 v221, v221\n"
"v_exp_f32 v222, v222\n"
"v_exp_f32 v223, v223\n"
"v_exp_f32 v224, v224\n"
"v_exp_f32 v225, v225\n"
"v_smfmac_f32_32x32x32_bf16 v[144:159], a[180:183], v[216:223], v45\n"
"v_exp_f32 v226, v226\n"
"v_exp_f32 v227, v227\n"
"v_exp_f32 v228, v228\n"
"v_mfma_f32_32x32x16_bf16 v[208:223], a[172:175], v[136:139], 0\n"
"v_exp_f32 v229, v229\n"
"v_exp_f32 v230, v230\n"
"v_exp_f32 v231, v231\n"
"v_exp_f32 v232, v232\n"
"v_exp_f32 v233, v233\n"
"v_smfmac_f32_32x32x32_bf16 v[160:175], a[176:179], v[224:231], v45\n"
"s_mov_b64 exec, s[76:77]\n"
"buffer_load_dwordx4 a[200:203], v47, s[40:43], s0 offen offset:3072 sc1\n"
"buffer_load_dwordx4 a[204:207], v47, s[40:43], s0 offen offset:3584 sc1\n"
"s_mov_b64 exec, -1\n"
"buffer_load_dwordx4 a[196:199], v2, s[40:43], s0 offen offset:2048 sc1\n"
"v_exp_f32 v234, v234\n"
"v_exp_f32 v235, v235\n"
"v_exp_f32 v236, v236\n"
"v_exp_f32 v237, v237\n"
"v_exp_f32 v238, v238\n"
"v_exp_f32 v239, v239\n"
"v_exp_f32 v208, v208\n"
"v_exp_f32 v209, v209\n"
"v_smfmac_f32_32x32x32_bf16 v[160:175], a[180:183], v[232:239], v45\n"
"v_exp_f32 v210, v210\n"
"v_exp_f32 v211, v211\n"
"v_exp_f32 v212, v212\n"
"v_mfma_f32_32x32x16_bf16 v[224:239], a[172:175], v[140:143], 0\n"
"s_xor_b32 s0, s0, 16384\n"
"v_exp_f32 v213, v213\n"
"v_exp_f32 v214, v214\n"
"v_exp_f32 v215, v215\n"
"v_exp_f32 v216, v216\n"
"v_exp_f32 v217, v217\n"
"v_smfmac_f32_32x32x32_bf16 v[176:191], a[176:179], v[208:215], v45\n"
"s_mov_b64 exec, s[76:77]\n"
"buffer_load_dwordx4 a[212:215], v47, s[40:43], s0 offen offset:1024 sc1\n"
"buffer_load_dwordx4 a[216:219], v47, s[40:43], s0 offen offset:1536 sc1\n"
"buffer_load_dwordx4 a[224:227], v47, s[40:43], s0 offen offset:3072 sc1\n"
"buffer_load_dwordx4 a[228:231], v47, s[40:43], s0 offen offset:3584 sc1\n"
"s_mov_b64 exec, -1\n"
"v_exp_f32 v218, v218\n"
"v_exp_f32 v219, v219\n"
"v_exp_f32 v220, v220\n"
"v_exp_f32 v221, v221\n"
"v_exp_f32 v222, v222\n"
"v_exp_f32 v223, v223\n"
"v_exp_f32 v224, v224\n"
"v_exp_f32 v225, v225\n"
"v_smfmac_f32_32x32x32_bf16 v[176:191], a[180:183], v[216:223], v45\n"
"s_waitcnt vmcnt(7)\n"
"v_exp_f32 v226, v226\n"
"v_exp_f32 v227, v227\n"
"v_exp_f32 v228, v228\n"
"v_mfma_f32_32x32x16_bf16 v[208:223], a[184:187], v[128:131], 0\n"
"v_exp_f32 v229, v229\n"
"v_exp_f32 v230, v230\n"
"v_exp_f32 v231, v231\n"
"v_exp_f32 v232, v232\n"
"v_exp_f32 v233, v233\n"
"v_smfmac_f32_32x32x32_bf16 v[192:207], a[176:179], v[224:231], v45\n"
"buffer_load_dwordx4 a[208:211], v2, s[40:43], s0 offen offset:0 sc1\n"
"buffer_load_dwordx4 a[220:223], v2, s[40:43], s0 offen offset:2048 sc1\n"
"v_exp_f32 v234, v234\n"
"v_exp_f32 v235, v235\n"
"v_exp_f32 v236, v236\n"
"v_exp_f32 v237, v237\n"
"v_exp_f32 v238, v238\n"
"v_exp_f32 v239, v239\n"
"v_exp_f32 v208, v208\n"
"v_exp_f32 v209, v209\n"
"v_smfmac_f32_32x32x32_bf16 v[192:207], a[180:183], v[232:239], v45\n"
"v_exp_f32 v210, v210\n"
"v_exp_f32 v211, v211\n"
"v_exp_f32 v212, v212\n"
"v_mfma_f32_32x32x16_bf16 v[224:239], a[184:187], v[132:135], 0\n"
"s_xor_b32 s0, s0, 8192\n"
"v_exp_f32 v213, v213\n"
"v_exp_f32 v214, v214\n"
"v_exp_f32 v215, v215\n"
"v_exp_f32 v216, v216\n"
"v_exp_f32 v217, v217\n"
"v_smfmac_f32_32x32x32_bf16 v[144:159], a[188:191], v[208:215], v45\n"
"s_mov_b64 exec, s[76:77]\n"
"buffer_load_dwordx4 a[236:239], v47, s[40:43], s0 offen offset:1024 sc1\n"
"buffer_load_dwordx4 a[240:243], v47, s[40:43], s0 offen offset:1536 sc1\n"
"buffer_load_dwordx4 a[248:251], v47, s[40:43], s0 offen offset:3072 sc1\n"
"buffer_load_dwordx4 a[252:255], v47, s[40:43], s0 offen offset:3584 sc1\n"
"s_mov_b64 exec, -1\n"
"v_exp_f32 v218, v218\n"
"v_exp_f32 v219, v219\n"
"v_exp_f32 v220, v220\n"
"v_exp_f32 v221, v221\n"
"v_exp_f32 v222, v222\n"
"v_exp_f32 v223, v223\n"
"v_exp_f32 v224, v224\n"
"v_exp_f32 v225, v225\n"
"v_smfmac_f32_32x32x32_bf16 v[144:159], a[192:195], v[216:223], v45\n"
"v_exp_f32 v226, v226\n"
"v_exp_f32 v227, v227\n"
"v_exp_f32 v228, v228\n"
"v_mfma_f32_32x32x16_bf16 v[208:223], a[184:187], v[136:139], 0\n"
"v_exp_f32 v229, v229\n"
"v_exp_f32 v230, v230\n"
"v_exp_f32 v231, v231\n"
"v_exp_f32 v232, v232\n"
"v_exp_f32 v233, v233\n"
"v_smfmac_f32_32x32x32_bf16 v[160:175], a[188:191], v[224:231], v45\n"
"buffer_load_dwordx4 a[232:235], v2, s[40:43], s0 offen offset:0 sc1\n"
"buffer_load_dwordx4 a[244:247], v2, s[40:43], s0 offen offset:2048 sc1\n"
"v_exp_f32 v234, v234\n"
"v_exp_f32 v235, v235\n"
"v_exp_f32 v236, v236\n"
"v_exp_f32 v237, v237\n"
"v_exp_f32 v238, v238\n"
"v_exp_f32 v239, v239\n"
"v_exp_f32 v208, v208\n"
"v_exp_f32 v209, v209\n"
"v_smfmac_f32_32x32x32_bf16 v[160:175], a[192:195], v[232:239], v45\n"
"s_add_u32 m0, s67, 0\n"
"v_exp_f32 v210, v210\n"
"v_exp_f32 v211, v211\n"
"v_exp_f32 v212, v212\n"
"v_mfma_f32_32x32x16_bf16 v[224:239], a[184:187], v[140:143], 0\n"
"v_exp_f32 v213, v213\n"
"v_exp_f32 v214, v214\n"
"v_exp_f32 v215, v215\n"
"v_exp_f32 v216, v216\n"
"v_exp_f32 v217, v217\n"
"v_smfmac_f32_32x32x32_bf16 v[176:191], a[188:191], v[208:215], v45\n"
"buffer_load_dwordx4 v46, s[44:47], s72 offen lds\n"
"v_exp_f32 v218, v218\n"
"v_exp_f32 v219, v219\n"
"v_exp_f32 v220, v220\n"
"v_exp_f32 v221, v221\n"
"v_exp_f32 v222, v222\n"
"v_exp_f32 v223, v223\n"
"v_exp_f32 v224, v224\n"
"v_exp_f32 v225, v225\n"
"v_smfmac_f32_32x32x32_bf16 v[176:191], a[192:195], v[216:223], v45\n"
"s_waitcnt vmcnt(13)\n"
"s_add_u32 m0, s67, 1024\n"
"v_exp_f32 v226, v226\n"
"v_exp_f32 v227, v227\n"
"v_exp_f32 v228, v228\n"
"v_mfma_f32_32x32x16_bf16 v[208:223], a[196:199], v[128:131], 0\n"
"v_exp_f32 v229, v229\n"
"v_exp_f32 v230, v230\n"
"v_exp_f32 v231, v231\n"
"v_exp_f32 v232, v232\n"
"v_exp_f32 v233, v233\n"
"v_smfmac_f32_32x32x32_bf16 v[192:207], a[188:191], v[224:231], v45\n"
"buffer_load_dwordx4 v46, s[44:47], s73 offen lds\n"
"v_exp_f32 v234, v234\n"
"v_exp_f32 v235, v235\n"
"v_exp_f32 v236, v236\n"
"v_exp_f32 v237, v237\n"
"v_exp_f32 v238, v238\n"
"v_exp_f32 v239, v239\n"
"v_exp_f32 v208, v208\n"
"v_exp_f32 v209, v209\n"
"v_smfmac_f32_32x32x32_bf16 v[192:207], a[192:195], v[232:239], v45\n"
"s_add_u32 m0, s67, 2048\n"
"v_exp_f32 v210, v210\n"
"v_exp_f32 v211, v211\n"
"v_exp_f32 v212, v212\n"
"v_mfma_f32_32x32x16_bf16 v[224:239], a[196:199], v[132:135], 0\n"
"v_exp_f32 v213, v213\n"
"v_exp_f32 v214, v214\n"
"v_exp_f32 v215, v215\n"
"v_exp_f32 v216, v216\n"
"v_exp_f32 v217, v217\n"
"v_smfmac_f32_32x32x32_bf16 v[144:159], a[200:203], v[208:215], v45\n"
"buffer_load_dwordx4 v46, s[44:47], s74 offen lds\n"
"v_exp_f32 v218, v218\n"
"v_exp_f32 v219, v219\n"
"v_exp_f32 v220, v220\n"
"v_exp_f32 v221, v221\n"
"v_exp_f32 v222, v222\n"
"v_exp_f32 v223, v223\n"
"v_exp_f32 v224, v224\n"
"v_exp_f32 v225, v225\n"
"v_smfmac_f32_32x32x32_bf16 v[144:159], a[204:207], v[216:223], v45\n"
"s_add_u32 m0, s67, 3072\n"
"v_exp_f32 v226, v226\n"
"v_exp_f32 v227, v227\n"
"v_exp_f32 v228, v228\n"
"v_mfma_f32_32x32x16_bf16 v[208:223], a[196:199], v[136:139], 0\n"
"v_exp_f32 v229, v229\n"
"v_exp_f32 v230, v230\n"
"v_exp_f32 v231, v231\n"
"v_exp_f32 v232, v232\n"
"v_exp_f32 v233, v233\n"
"v_smfmac_f32_32x32x32_bf16 v[160:175], a[200:203], v[224:231], v45\n"
"buffer_load_dwordx4 v46, s[44:47], s75 offen lds\n"
"v_exp_f32 v234, v234\n"
"v_exp_f32 v235, v235\n"
"v_exp_f32 v236, v236\n"
"v_exp_f32 v237, v237\n"
"v_exp_f32 v238, v238\n"
"v_exp_f32 v239, v239\n"
"v_exp_f32 v208, v208\n"
"v_exp_f32 v209, v209\n"
"v_smfmac_f32_32x32x32_bf16 v[160:175], a[204:207], v[232:239], v45\n"
"v_exp_f32 v210, v210\n"
"v_exp_f32 v211, v211\n"
"v_exp_f32 v212, v212\n"
"v_mfma_f32_32x32x16_bf16 v[224:239], a[196:199], v[140:143], 0\n"
"v_exp_f32 v213, v213\n"
"v_exp_f32 v214, v214\n"
"v_exp_f32 v215, v215\n"
"v_exp_f32 v216, v216\n"
"v_exp_f32 v217, v217\n"
"v_smfmac_f32_32x32x32_bf16 v[176:191], a[200:203], v[208:215], v45\n"
"v_exp_f32 v218, v218\n"
"v_exp_f32 v219, v219\n"
"v_exp_f32 v220, v220\n"
"v_exp_f32 v221, v221\n"
"v_exp_f32 v222, v222\n"
"v_exp_f32 v223, v223\n"
"v_exp_f32 v224, v224\n"
"v_exp_f32 v225, v225\n"
"v_smfmac_f32_32x32x32_bf16 v[176:191], a[204:207], v[216:223], v45\n"
"s_waitcnt vmcnt(11)\n"
"v_exp_f32 v226, v226\n"
"v_exp_f32 v227, v227\n"
"v_exp_f32 v228, v228\n"
"v_mfma_f32_32x32x16_bf16 v[208:223], a[208:211], v[128:131], 0\n"
"v_exp_f32 v229, v229\n"
"v_exp_f32 v230, v230\n"
"v_exp_f32 v231, v231\n"
"v_exp_f32 v232, v232\n"
"v_exp_f32 v233, v233\n"
"v_smfmac_f32_32x32x32_bf16 v[192:207], a[200:203], v[224:231], v45\n"
"v_exp_f32 v234, v234\n"
"v_exp_f32 v235, v235\n"
"v_exp_f32 v236, v236\n"
"v_exp_f32 v237, v237\n"
"v_exp_f32 v238, v238\n"
"v_exp_f32 v239, v239\n"
"v_exp_f32 v208, v208\n"
"v_exp_f32 v209, v209\n"
"v_smfmac_f32_32x32x32_bf16 v[192:207], a[204:207], v[232:239], v45\n"
"v_exp_f32 v210, v210\n"
"v_exp_f32 v211, v211\n"
"v_exp_f32 v212, v212\n"
"v_mfma_f32_32x32x16_bf16 v[224:239], a[208:211], v[132:135], 0\n"
"v_exp_f32 v213, v213\n"
"v_exp_f32 v214, v214\n"
"v_exp_f32 v215, v215\n"
"v_exp_f32 v216, v216\n"
"v_exp_f32 v217, v217\n"
"v_smfmac_f32_32x32x32_bf16 v[144:159], a[212:215], v[208:215], v45\n"
"v_exp_f32 v218, v218\n"
"v_exp_f32 v219, v219\n"
"v_exp_f32 v220, v220\n"
"v_exp_f32 v221, v221\n"
"v_exp_f32 v222, v222\n"
"v_exp_f32 v223, v223\n"
"v_exp_f32 v224, v224\n"
"v_exp_f32 v225, v225\n"
"v_smfmac_f32_32x32x32_bf16 v[144:159], a[216:219], v[216:223], v45\n"
"v_exp_f32 v226, v226\n"
"v_exp_f32 v227, v227\n"
"v_exp_f32 v228, v228\n"
"v_mfma_f32_32x32x16_bf16 v[208:223], a[208:211], v[136:139], 0\n"
"v_exp_f32 v229, v229\n"
"v_exp_f32 v230, v230\n"
"v_exp_f32 v231, v231\n"
"v_exp_f32 v232, v232\n"
"v_exp_f32 v233, v233\n"
"v_smfmac_f32_32x32x32_bf16 v[160:175], a[212:215], v[224:231], v45\n"
"v_exp_f32 v234, v234\n"
"v_exp_f32 v235, v235\n"
"v_exp_f32 v236, v236\n"
"v_exp_f32 v237, v237\n"
"v_exp_f32 v238, v238\n"
"v_exp_f32 v239, v239\n"
"v_exp_f32 v208, v208\n"
"v_exp_f32 v209, v209\n"
"v_smfmac_f32_32x32x32_bf16 v[160:175], a[216:219], v[232:239], v45\n"
"v_exp_f32 v210, v210\n"
"v_exp_f32 v211, v211\n"
"v_exp_f32 v212, v212\n"
"v_mfma_f32_32x32x16_bf16 v[224:239], a[208:211], v[140:143], 0\n"
"s_xor_b32 s0, s0, 32768\n"
"v_exp_f32 v213, v213\n"
"v_exp_f32 v214, v214\n"
"v_exp_f32 v215, v215\n"
"v_exp_f32 v216, v216\n"
"v_exp_f32 v217, v217\n"
"v_smfmac_f32_32x32x32_bf16 v[176:191], a[212:215], v[208:215], v45\n"
"s_mov_b64 exec, s[76:77]\n"
"buffer_load_dwordx4 a[164:167], v47, s[40:43], s0 offen offset:1024 sc1\n"
"buffer_load_dwordx4 a[168:171], v47, s[40:43], s0 offen offset:1536 sc1\n"
"buffer_load_dwordx4 a[176:179], v47, s[40:43], s0 offen offset:3072 sc1\n"
"buffer_load_dwordx4 a[180:183], v47, s[40:43], s0 offen offset:3584 sc1\n"
"s_mov_b64 exec, -1\n"
"v_exp_f32 v218, v218\n"
"v_exp_f32 v219, v219\n"
"v_exp_f32 v220, v220\n"
"v_exp_f32 v221, v221\n"
"v_exp_f32 v222, v222\n"
"v_exp_f32 v223, v223\n"
"v_exp_f32 v224, v224\n"
"v_exp_f32 v225, v225\n"
"v_smfmac_f32_32x32x32_bf16 v[176:191], a[216:219], v[216:223], v45\n"
"s_waitcnt vmcnt(14)\n"
"v_exp_f32 v226, v226\n"
"v_exp_f32 v227, v227\n"
"v_exp_f32 v228, v228\n"
"v_mfma_f32_32x32x16_bf16 v[208:223], a[220:223], v[128:131], 0\n"
"v_exp_f32 v229, v229\n"
"v_exp_f32 v230, v230\n"
"v_exp_f32 v231, v231\n"
"v_exp_f32 v232, v232\n"
"v_exp_f32 v233, v233\n"
"v_smfmac_f32_32x32x32_bf16 v[192:207], a[212:215], v[224:231], v45\n"
"v_exp_f32 v234, v234\n"
"v_exp_f32 v235, v235\n"
"v_exp_f32 v236, v236\n"
"v_exp_f32 v237, v237\n"
"v_exp_f32 v238, v238\n"
"v_exp_f32 v239, v239\n"
"v_exp_f32 v208, v208\n"
"v_exp_f32 v209, v209\n"
"v_smfmac_f32_32x32x32_bf16 v[192:207], a[216:219], v[232:239], v45\n"
"v_exp_f32 v210, v210\n"
"v_exp_f32 v211, v211\n"
"v_exp_f32 v212, v212\n"
"v_mfma_f32_32x32x16_bf16 v[224:239], a[220:223], v[132:135], 0\n"
"v_exp_f32 v213, v213\n"
"v_exp_f32 v214, v214\n"
"v_exp_f32 v215, v215\n"
"v_exp_f32 v216, v216\n"
"v_exp_f32 v217, v217\n"
"v_smfmac_f32_32x32x32_bf16 v[144:159], a[224:227], v[208:215], v45\n"
"v_exp_f32 v218, v218\n"
"v_exp_f32 v219, v219\n"
"v_exp_f32 v220, v220\n"
"v_exp_f32 v221, v221\n"
"v_exp_f32 v222, v222\n"
"v_exp_f32 v223, v223\n"
"v_exp_f32 v224, v224\n"
"v_exp_f32 v225, v225\n"
"v_smfmac_f32_32x32x32_bf16 v[144:159], a[228:231], v[216:223], v45\n"
"v_exp_f32 v226, v226\n"
"v_exp_f32 v227, v227\n"
"v_exp_f32 v228, v228\n"
"v_mfma_f32_32x32x16_bf16 v[208:223], a[220:223], v[136:139], 0\n"
"v_exp_f32 v229, v229\n"
"v_exp_f32 v230, v230\n"
"v_exp_f32 v231, v231\n"
"v_exp_f32 v232, v232\n"
"v_exp_f32 v233, v233\n"
"v_smfmac_f32_32x32x32_bf16 v[160:175], a[224:227], v[224:231], v45\n"
"v_exp_f32 v234, v234\n"
"v_exp_f32 v235, v235\n"
"v_exp_f32 v236, v236\n"
"v_exp_f32 v237, v237\n"
"v_exp_f32 v238, v238\n"
"v_exp_f32 v239, v239\n"
"v_exp_f32 v208, v208\n"
"v_exp_f32 v209, v209\n"
"v_smfmac_f32_32x32x32_bf16 v[160:175], a[228:231], v[232:239], v45\n"
"v_exp_f32 v210, v210\n"
"v_exp_f32 v211, v211\n"
"v_exp_f32 v212, v212\n"
"v_mfma_f32_32x32x16_bf16 v[224:239], a[220:223], v[140:143], 0\n"
"v_exp_f32 v213, v213\n"
"v_exp_f32 v214, v214\n"
"v_exp_f32 v215, v215\n"
"v_exp_f32 v216, v216\n"
"v_exp_f32 v217, v217\n"
"v_smfmac_f32_32x32x32_bf16 v[176:191], a[224:227], v[208:215], v45\n"
"buffer_load_dwordx4 a[160:163], v2, s[40:43], s0 offen offset:0 sc1\n"
"buffer_load_dwordx4 a[172:175], v2, s[40:43], s0 offen offset:2048 sc1\n"
"v_exp_f32 v218, v218\n"
"v_exp_f32 v219, v219\n"
"v_exp_f32 v220, v220\n"
"v_exp_f32 v221, v221\n"
"v_exp_f32 v222, v222\n"
"v_exp_f32 v223, v223\n"
"v_exp_f32 v224, v224\n"
"v_exp_f32 v225, v225\n"
"v_smfmac_f32_32x32x32_bf16 v[176:191], a[228:231], v[216:223], v45\n"
"s_waitcnt vmcnt(11)\n"
"v_exp_f32 v226, v226\n"
"v_exp_f32 v227, v227\n"
"v_exp_f32 v228, v228\n"
"v_mfma_f32_32x32x16_bf16 v[208:223], a[232:235], v[128:131], 0\n"
"v_exp_f32 v229, v229\n"
"v_exp_f32 v230, v230\n"
"v_exp_f32 v231, v231\n"
"v_exp_f32 v232, v232\n"
"v_exp_f32 v233, v233\n"
"v_smfmac_f32_32x32x32_bf16 v[192:207], a[224:227], v[224:231], v45\n"
"v_exp_f32 v234, v234\n"
"v_exp_f32 v235, v235\n"
"v_exp_f32 v236, v236\n"
"v_exp_f32 v237, v237\n"
"v_exp_f32 v238, v238\n"
"v_exp_f32 v239, v239\n"
"v_exp_f32 v208, v208\n"
"v_exp_f32 v209, v209\n"
"v_smfmac_f32_32x32x32_bf16 v[192:207], a[228:231], v[232:239], v45\n"
"v_exp_f32 v210, v210\n"
"v_exp_f32 v211, v211\n"
"v_exp_f32 v212, v212\n"
"v_mfma_f32_32x32x16_bf16 v[224:239], a[232:235], v[132:135], 0\n"
"s_xor_b32 s0, s0, 8192\n"
"v_exp_f32 v213, v213\n"
"v_exp_f32 v214, v214\n"
"v_exp_f32 v215, v215\n"
"v_exp_f32 v216, v216\n"
"v_exp_f32 v217, v217\n"
"v_smfmac_f32_32x32x32_bf16 v[144:159], a[236:239], v[208:215], v45\n"
"s_mov_b64 exec, s[76:77]\n"
"buffer_load_dwordx4 a[188:191], v47, s[40:43], s0 offen offset:1024 sc1\n"
"buffer_load_dwordx4 a[192:195], v47, s[40:43], s0 offen offset:1536 sc1\n"
"buffer_load_dwordx4 a[200:203], v47, s[40:43], s0 offen offset:3072 sc1\n"
"buffer_load_dwordx4 a[204:207], v47, s[40:43], s0 offen offset:3584 sc1\n"
"s_mov_b64 exec, -1\n"
"v_exp_f32 v218, v218\n"
"v_exp_f32 v219, v219\n"
"v_exp_f32 v220, v220\n"
"v_exp_f32 v221, v221\n"
"v_exp_f32 v222, v222\n"
"v_exp_f32 v223, v223\n"
"v_exp_f32 v224, v224\n"
"v_exp_f32 v225, v225\n"
"v_smfmac_f32_32x32x32_bf16 v[144:159], a[240:243], v[216:223], v45\n"
"v_exp_f32 v226, v226\n"
"v_exp_f32 v227, v227\n"
"v_exp_f32 v228, v228\n"
"v_mfma_f32_32x32x16_bf16 v[208:223], a[232:235], v[136:139], 0\n"
"v_exp_f32 v229, v229\n"
"v_exp_f32 v230, v230\n"
"v_exp_f32 v231, v231\n"
"v_exp_f32 v232, v232\n"
"v_exp_f32 v233, v233\n"
"v_smfmac_f32_32x32x32_bf16 v[160:175], a[236:239], v[224:231], v45\n"
"v_exp_f32 v234, v234\n"
"v_exp_f32 v235, v235\n"
"v_exp_f32 v236, v236\n"
"v_exp_f32 v237, v237\n"
"v_exp_f32 v238, v238\n"
"v_exp_f32 v239, v239\n"
"v_exp_f32 v208, v208\n"
"v_exp_f32 v209, v209\n"
"v_smfmac_f32_32x32x32_bf16 v[160:175], a[240:243], v[232:239], v45\n"
"v_exp_f32 v210, v210\n"
"v_exp_f32 v211, v211\n"
"v_exp_f32 v212, v212\n"
"v_mfma_f32_32x32x16_bf16 v[224:239], a[232:235], v[140:143], 0\n"
"v_exp_f32 v213, v213\n"
"v_exp_f32 v214, v214\n"
"v_exp_f32 v215, v215\n"
"v_exp_f32 v216, v216\n"
"v_exp_f32 v217, v217\n"
"v_smfmac_f32_32x32x32_bf16 v[176:191], a[236:239], v[208:215], v45\n"
"v_exp_f32 v218, v218\n"
"v_exp_f32 v219, v219\n"
"v_exp_f32 v220, v220\n"
"v_exp_f32 v221, v221\n"
"v_exp_f32 v222, v222\n"
"v_exp_f32 v223, v223\n"
"v_exp_f32 v224, v224\n"
"v_exp_f32 v225, v225\n"
"v_smfmac_f32_32x32x32_bf16 v[176:191], a[240:243], v[216:223], v45\n"
"s_waitcnt vmcnt(14)\n"
"v_exp_f32 v226, v226\n"
"v_exp_f32 v227, v227\n"
"v_exp_f32 v228, v228\n"
"v_mfma_f32_32x32x16_bf16 v[208:223], a[244:247], v[128:131], 0\n"
"v_exp_f32 v229, v229\n"
"v_exp_f32 v230, v230\n"
"v_exp_f32 v231, v231\n"
"v_exp_f32 v232, v232\n"
"v_exp_f32 v233, v233\n"
"v_smfmac_f32_32x32x32_bf16 v[192:207], a[236:239], v[224:231], v45\n"
"v_exp_f32 v234, v234\n"
"v_exp_f32 v235, v235\n"
"v_exp_f32 v236, v236\n"
"v_exp_f32 v237, v237\n"
"v_exp_f32 v238, v238\n"
"v_exp_f32 v239, v239\n"
"v_exp_f32 v208, v208\n"
"v_exp_f32 v209, v209\n"
"v_smfmac_f32_32x32x32_bf16 v[192:207], a[240:243], v[232:239], v45\n"
"s_waitcnt vmcnt(10)\n"
"ds_read_b64_tr_b16 v[48:49], v240\n"
"ds_read_b64_tr_b16 v[50:51], v244\n"
"v_exp_f32 v210, v210\n"
"v_exp_f32 v211, v211\n"
"v_exp_f32 v212, v212\n"
"v_mfma_f32_32x32x16_bf16 v[224:239], a[244:247], v[132:135], 0\n"
"v_exp_f32 v213, v213\n"
"v_exp_f32 v214, v214\n"
"v_exp_f32 v215, v215\n"
"v_exp_f32 v216, v216\n"
"v_exp_f32 v217, v217\n"
"v_smfmac_f32_32x32x32_bf16 v[144:159], a[248:251], v[208:215], v45\n"
"buffer_load_dwordx4 a[184:187], v2, s[40:43], s0 offen offset:0 sc1\n"
"buffer_load_dwordx4 a[196:199], v2, s[40:43], s0 offen offset:2048 sc1\n"
"s_waitcnt lgkmcnt(0)\n"
"buffer_store_dwordx4 v[48:51], v2, s[40:43], s18 offen sc1\n"
"v_exp_f32 v218, v218\n"
"v_exp_f32 v219, v219\n"
"v_exp_f32 v220, v220\n"
"v_exp_f32 v221, v221\n"
"v_exp_f32 v222, v222\n"
"v_exp_f32 v223, v223\n"
"v_exp_f32 v224, v224\n"
"v_exp_f32 v225, v225\n"
"v_smfmac_f32_32x32x32_bf16 v[144:159], a[252:255], v[216:223], v45\n"
"ds_read_b64_tr_b16 v[52:53], v241\n"
"ds_read_b64_tr_b16 v[54:55], v245\n"
"v_exp_f32 v226, v226\n"
"v_exp_f32 v227, v227\n"
"v_exp_f32 v228, v228\n"
"v_mfma_f32_32x32x16_bf16 v[208:223], a[244:247], v[136:139], 0\n"
"v_exp_f32 v229, v229\n"
"v_exp_f32 v230, v230\n"
"v_exp_f32 v231, v231\n"
"v_exp_f32 v232, v232\n"
"v_exp_f32 v233, v233\n"
"v_smfmac_f32_32x32x32_bf16 v[160:175], a[248:251], v[224:231], v45\n"
"s_waitcnt lgkmcnt(0)\n"
"buffer_store_dwordx4 v[52:55], v2, s[40:43], s19 offen sc1\n"
"v_exp_f32 v234, v234\n"
"v_exp_f32 v235, v235\n"
"v_exp_f32 v236, v236\n"
"v_exp_f32 v237, v237\n"
"v_exp_f32 v238, v238\n"
"v_exp_f32 v239, v239\n"
"v_exp_f32 v208, v208\n"
"v_exp_f32 v209, v209\n"
"v_smfmac_f32_32x32x32_bf16 v[160:175], a[252:255], v[232:239], v45\n"
"ds_read_b64_tr_b16 v[56:57], v242\n"
"ds_read_b64_tr_b16 v[58:59], v246\n"
"v_exp_f32 v210, v210\n"
"v_exp_f32 v211, v211\n"
"v_exp_f32 v212, v212\n"
"v_mfma_f32_32x32x16_bf16 v[224:239], a[244:247], v[140:143], 0\n"
"s_xor_b32 s0, s0, 16384\n"
"v_exp_f32 v213, v213\n"
"v_exp_f32 v214, v214\n"
"v_exp_f32 v215, v215\n"
"v_exp_f32 v216, v216\n"
"v_exp_f32 v217, v217\n"
"v_smfmac_f32_32x32x32_bf16 v[176:191], a[248:251], v[208:215], v45\n"
"s_mov_b64 exec, s[76:77]\n"
"buffer_load_dwordx4 a[212:215], v47, s[40:43], s0 offen offset:1024 sc1\n"
"buffer_load_dwordx4 a[216:219], v47, s[40:43], s0 offen offset:1536 sc1\n"
"buffer_load_dwordx4 a[224:227], v47, s[40:43], s0 offen offset:3072 sc1\n"
"buffer_load_dwordx4 a[228:231], v47, s[40:43], s0 offen offset:3584 sc1\n"
"s_mov_b64 exec, -1\n"
"s_waitcnt lgkmcnt(0)\n"
"buffer_store_dwordx4 v[56:59], v2, s[40:43], s20 offen sc1\n"
"v_exp_f32 v218, v218\n"
"v_exp_f32 v219, v219\n"
"v_exp_f32 v220, v220\n"
"v_exp_f32 v221, v221\n"
"v_exp_f32 v222, v222\n"
"v_exp_f32 v223, v223\n"
"v_exp_f32 v224, v224\n"
"v_exp_f32 v225, v225\n"
"v_smfmac_f32_32x32x32_bf16 v[176:191], a[252:255], v[216:223], v45\n"
"s_waitcnt vmcnt(14)\n"
"ds_read_b64_tr_b16 v[60:61], v243\n"
"ds_read_b64_tr_b16 v[62:63], v247\n"
"v_exp_f32 v226, v226\n"
"v_exp_f32 v227, v227\n"
"L_pad5:\n"
"s_cbranch_execz L_ret10\n"
"v_exp_f32 v228, v228\n"
"v_mfma_f32_32x32x16_bf16 v[208:223], a[160:163], v[128:131], 0\n"
"v_exp_f32 v229, v229\n"
"v_exp_f32 v230, v230\n"
"v_exp_f32 v231, v231\n"
"v_exp_f32 v232, v232\n"
"v_exp_f32 v233, v233\n"
"v_smfmac_f32_32x32x32_bf16 v[192:207], a[248:251], v[224:231], v45\n"
"s_waitcnt lgkmcnt(0)\n"
"buffer_store_dwordx4 v[60:63], v2, s[40:43], s21 offen sc1\n"
"v_exp_f32 v234, v234\n"
"v_exp_f32 v235, v235\n"
"v_exp_f32 v236, v236\n"
"v_exp_f32 v237, v237\n"
"v_exp_f32 v238, v238\n"
"v_exp_f32 v239, v239\n"
"v_exp_f32 v208, v208\n"
"v_exp_f32 v209, v209\n"
"v_smfmac_f32_32x32x32_bf16 v[192:207], a[252:255], v[232:239], v45\n"
"v_exp_f32 v210, v210\n"
"v_exp_f32 v211, v211\n"
"v_exp_f32 v212, v212\n"
"v_mfma_f32_32x32x16_bf16 v[224:239], a[160:163], v[132:135], 0\n"
"v_exp_f32 v213, v213\n"
"v_exp_f32 v214, v214\n"
"v_exp_f32 v215, v215\n"
"v_exp_f32 v216, v216\n"
"v_exp_f32 v217, v217\n"
"v_smfmac_f32_32x32x32_bf16 v[144:159], a[164:167], v[208:215], v45\n"
"v_exp_f32 v218, v218\n"
"v_exp_f32 v219, v219\n"
"v_exp_f32 v220, v220\n"
"v_exp_f32 v221, v221\n"
"v_exp_f32 v222, v222\n"
"v_exp_f32 v223, v223\n"
"v_exp_f32 v224, v224\n"
"v_exp_f32 v225, v225\n"
"v_smfmac_f32_32x32x32_bf16 v[144:159], a[168:171], v[216:223], v45\n"
"v_exp_f32 v226, v226\n"
"v_exp_f32 v227, v227\n"
"v_exp_f32 v228, v228\n"
"v_mfma_f32_32x32x16_bf16 v[208:223], a[160:163], v[136:139], 0\n"
"v_exp_f32 v229, v229\n"
"v_exp_f32 v230, v230\n"
"v_exp_f32 v231, v231\n"
"v_exp_f32 v232, v232\n"
"v_exp_f32 v233, v233\n"
"v_smfmac_f32_32x32x32_bf16 v[160:175], a[164:167], v[224:231], v45\n"
"v_exp_f32 v234, v234\n"
"v_exp_f32 v235, v235\n"
"v_exp_f32 v236, v236\n"
"v_exp_f32 v237, v237\n"
"v_exp_f32 v238, v238\n"
"v_exp_f32 v239, v239\n"
"v_exp_f32 v208, v208\n"
"v_exp_f32 v209, v209\n"
"v_smfmac_f32_32x32x32_bf16 v[160:175], a[168:171], v[232:239], v45\n"
"v_exp_f32 v210, v210\n"
"v_exp_f32 v211, v211\n"
"v_exp_f32 v212, v212\n"
"v_mfma_f32_32x32x16_bf16 v[224:239], a[160:163], v[140:143], 0\n"
"v_exp_f32 v213, v213\n"
"v_exp_f32 v214, v214\n"
"v_exp_f32 v215, v215\n"
"v_exp_f32 v216, v216\n"
"v_exp_f32 v217, v217\n"
"v_smfmac_f32_32x32x32_bf16 v[176:191], a[164:167], v[208:215], v45\n"
"buffer_load_dwordx4 a[208:211], v2, s[40:43], s0 offen offset:0 sc1\n"
"buffer_load_dwordx4 a[220:223], v2, s[40:43], s0 offen offset:2048 sc1\n"
"v_exp_f32 v218, v218\n"
"v_exp_f32 v219, v219\n"
"v_exp_f32 v220, v220\n"
"v_exp_f32 v221, v221\n"
"v_exp_f32 v222, v222\n"
"v_exp_f32 v223, v223\n"
"v_exp_f32 v224, v224\n"
"v_exp_f32 v225, v225\n"
"v_smfmac_f32_32x32x32_bf16 v[176:191], a[168:171], v[216:223], v45\n"
"s_waitcnt vmcnt(16)\n"
"v_exp_f32 v226, v226\n"
"v_exp_f32 v227, v227\n"
"v_exp_f32 v228, v228\n"
"v_mfma_f32_32x32x16_bf16 v[208:223], a[172:175], v[128:131], 0\n"
"v_exp_f32 v229, v229\n"
"v_exp_f32 v230, v230\n"
"v_exp_f32 v231, v231\n"
"v_exp_f32 v232, v232\n"
"v_exp_f32 v233, v233\n"
"v_smfmac_f32_32x32x32_bf16 v[192:207], a[164:167], v[224:231], v45\n"
"v_exp_f32 v234, v234\n"
"v_exp_f32 v235, v235\n"
"v_exp_f32 v236, v236\n"
"v_exp_f32 v237, v237\n"
"v_exp_f32 v238, v238\n"
"v_exp_f32 v239, v239\n"
"v_exp_f32 v208, v208\n"
"v_exp_f32 v209, v209\n"
"v_smfmac_f32_32x32x32_bf16 v[192:207], a[168:171], v[232:239], v45\n"
"v_exp_f32 v210, v210\n"
"v_exp_f32 v211, v211\n"
"v_exp_f32 v212, v212\n"
"v_mfma_f32_32x32x16_bf16 v[224:239], a[172:175], v[132:135], 0\n"
"s_xor_b32 s0, s0, 8192\n"
"v_exp_f32 v213, v213\n"
"v_exp_f32 v214, v214\n"
"v_exp_f32 v215, v215\n"
"v_exp_f32 v216, v216\n"
"v_exp_f32 v217, v217\n"
"v_smfmac_f32_32x32x32_bf16 v[144:159], a[176:179], v[208:215], v45\n"
"s_mov_b64 exec, s[76:77]\n"
"buffer_load_dwordx4 a[236:239], v47, s[40:43], s0 offen offset:1024 sc1\n"
"buffer_load_dwordx4 a[240:243], v47, s[40:43], s0 offen offset:1536 sc1\n"
"buffer_load_dwordx4 a[248:251], v47, s[40:43], s0 offen offset:3072 sc1\n"
"buffer_load_dwordx4 a[252:255], v47, s[40:43], s0 offen offset:3584 sc1\n"
"s_mov_b64 exec, -1\n"
"v_exp_f32 v218, v218\n"
"v_exp_f32 v219, v219\n"
"v_exp_f32 v220, v220\n"
"v_exp_f32 v221, v221\n"
"v_exp_f32 v222, v222\n"
"v_exp_f32 v223, v223\n"
"v_exp_f32 v224, v224\n"
"v_exp_f32 v225, v225\n"
"v_smfmac_f32_32x32x32_bf16 v[144:159], a[180:183], v[216:223], v45\n"
"v_exp_f32 v226, v226\n"
"v_exp_f32 v227, v227\n"
"v_exp_f32 v228, v228\n"
"v_mfma_f32_32x32x16_bf16 v[208:223], a[172:175], v[136:139], 0\n"
"v_exp_f32 v229, v229\n"
"v_exp_f32 v230, v230\n"
"v_exp_f32 v231, v231\n"
"v_exp_f32 v232, v232\n"
"v_exp_f32 v233, v233\n"
"v_smfmac_f32_32x32x32_bf16 v[160:175], a[176:179], v[224:231], v45\n"
"v_exp_f32 v234, v234\n"
"v_exp_f32 v235, v235\n"
"v_exp_f32 v236, v236\n"
"v_exp_f32 v237, v237\n"
"v_exp_f32 v238, v238\n"
"v_exp_f32 v239, v239\n"
"v_exp_f32 v208, v208\n"
"v_exp_f32 v209, v209\n"
"v_smfmac_f32_32x32x32_bf16 v[160:175], a[180:183], v[232:239], v45\n"
"v_exp_f32 v210, v210\n"
"v_exp_f32 v211, v211\n"
"v_exp_f32 v212, v212\n"
"v_mfma_f32_32x32x16_bf16 v[224:239], a[172:175], v[140:143], 0\n"
"v_exp_f32 v213, v213\n"
"v_exp_f32 v214, v214\n"
"v_exp_f32 v215, v215\n"
"v_exp_f32 v216, v216\n"
"v_exp_f32 v217, v217\n"
"v_smfmac_f32_32x32x32_bf16 v[176:191], a[176:179], v[208:215], v45\n"
"v_exp_f32 v218, v218\n"
"v_exp_f32 v219, v219\n"
"v_exp_f32 v220, v220\n"
"v_exp_f32 v221, v221\n"
"v_exp_f32 v222, v222\n"
"v_exp_f32 v223, v223\n"
"v_exp_f32 v224, v224\n"
"v_exp_f32 v225, v225\n"
"v_smfmac_f32_32x32x32_bf16 v[176:191], a[180:183], v[216:223], v45\n"
"s_waitcnt vmcnt(15)\n"
"s_waitcnt vmcnt(6)\n"
"s_mov_b64 exec, 1\n"
"buffer_store_dwordx2 v[18:19], off, s[40:43], s7 offset:128 sc1\n"
"s_mov_b64 exec, -1\n"
"v_exp_f32 v226, v226\n"
"v_exp_f32 v227, v227\n"
"v_exp_f32 v228, v228\n"
"v_mfma_f32_32x32x16_bf16 v[208:223], a[184:187], v[128:131], 0\n"
"v_exp_f32 v229, v229\n"
"v_exp_f32 v230, v230\n"
"v_exp_f32 v231, v231\n"
"v_exp_f32 v232, v232\n"
"v_exp_f32 v233, v233\n"
"v_smfmac_f32_32x32x32_bf16 v[192:207], a[176:179], v[224:231], v45\n"
"v_exp_f32 v234, v234\n"
"v_exp_f32 v235, v235\n"
"v_exp_f32 v236, v236\n"
"v_exp_f32 v237, v237\n"
"v_exp_f32 v238, v238\n"
"v_exp_f32 v239, v239\n"
"v_exp_f32 v208, v208\n"
"v_exp_f32 v209, v209\n"
"v_smfmac_f32_32x32x32_bf16 v[192:207], a[180:183], v[232:239], v45\n"
"v_exp_f32 v210, v210\n"
"v_exp_f32 v211, v211\n"
"v_exp_f32 v212, v212\n"
"v_mfma_f32_32x32x16_bf16 v[224:239], a[184:187], v[132:135], 0\n"
"v_exp_f32 v213, v213\n"
"v_exp_f32 v214, v214\n"
"v_exp_f32 v215, v215\n"
"v_exp_f32 v216, v216\n"
"v_exp_f32 v217, v217\n"
"v_smfmac_f32_32x32x32_bf16 v[144:159], a[188:191], v[208:215], v45\n"
"buffer_load_dwordx4 a[232:235], v2, s[40:43], s0 offen offset:0 sc1\n"
"buffer_load_dwordx4 a[244:247], v2, s[40:43], s0 offen offset:2048 sc1\n"
"v_exp_f32 v218, v218\n"
"v_exp_f32 v219, v219\n"
"v_exp_f32 v220, v220\n"
"v_exp_f32 v221, v221\n"
"v_exp_f32 v222, v222\n"
"v_exp_f32 v223, v223\n"
"v_exp_f32 v224, v224\n"
"v_exp_f32 v225, v225\n"
"v_smfmac_f32_32x32x32_bf16 v[144:159], a[192:195], v[216:223], v45\n"
"v_exp_f32 v226, v226\n"
"v_exp_f32 v227, v227\n"
"v_exp_f32 v228, v228\n"
"v_mfma_f32_32x32x16_bf16 v[208:223], a[184:187], v[136:139], 0\n"
"v_exp_f32 v229, v229\n"
"v_exp_f32 v230, v230\n"
"v_exp_f32 v231, v231\n"
"v_exp_f32 v232, v232\n"
"v_exp_f32 v233, v233\n"
"v_smfmac_f32_32x32x32_bf16 v[160:175], a[188:191], v[224:231], v45\n"
"v_exp_f32 v234, v234\n"
"v_exp_f32 v235, v235\n"
"v_exp_f32 v236, v236\n"
"v_exp_f32 v237, v237\n"
"v_exp_f32 v238, v238\n"
"v_exp_f32 v239, v239\n"
"v_exp_f32 v208, v208\n"
"v_exp_f32 v209, v209\n"
"v_smfmac_f32_32x32x32_bf16 v[160:175], a[192:195], v[232:239], v45\n"
"v_exp_f32 v210, v210\n"
"v_exp_f32 v211, v211\n"
"v_exp_f32 v212, v212\n"
"v_mfma_f32_32x32x16_bf16 v[224:239], a[184:187], v[140:143], 0\n"
"s_xor_b32 s0, s0, 65536\n"
"v_exp_f32 v213, v213\n"
"v_exp_f32 v214, v214\n"
"v_exp_f32 v215, v215\n"
"v_exp_f32 v216, v216\n"
"v_exp_f32 v217, v217\n"
"v_smfmac_f32_32x32x32_bf16 v[176:191], a[188:191], v[208:215], v45\n"
"s_mov_b64 exec, s[76:77]\n"
"buffer_load_dwordx4 a[164:167], v47, s[40:43], s0 offen offset:1024 sc1\n"
"buffer_load_dwordx4 a[168:171], v47, s[40:43], s0 offen offset:1536 sc1\n"
"buffer_load_dwordx4 a[176:179], v47, s[40:43], s0 offen offset:3072 sc1\n"
"buffer_load_dwordx4 a[180:183], v47, s[40:43], s0 offen offset:3584 sc1\n"
"s_mov_b64 exec, -1\n"
"v_exp_f32 v218, v218\n"
"v_exp_f32 v219, v219\n"
"v_exp_f32 v220, v220\n"
"v_exp_f32 v221, v221\n"
"v_exp_f32 v222, v222\n"
"v_exp_f32 v223, v223\n"
"v_exp_f32 v224, v224\n"
"v_exp_f32 v225, v225\n"
"v_smfmac_f32_32x32x32_bf16 v[176:191], a[192:195], v[216:223], v45\n"
"v_exp_f32 v226, v226\n"
"v_exp_f32 v227, v227\n"
"v_exp_f32 v228, v228\n"
"v_mfma_f32_32x32x16_bf16 v[208:223], a[196:199], v[128:131], 0\n"
"v_exp_f32 v229, v229\n"
"v_exp_f32 v230, v230\n"
"v_exp_f32 v231, v231\n"
"v_exp_f32 v232, v232\n"
"v_exp_f32 v233, v233\n"
"v_smfmac_f32_32x32x32_bf16 v[192:207], a[188:191], v[224:231], v45\n"
"v_exp_f32 v234, v234\n"
"v_exp_f32 v235, v235\n"
"v_exp_f32 v236, v236\n"
"v_exp_f32 v237, v237\n"
"v_exp_f32 v238, v238\n"
"v_exp_f32 v239, v239\n"
"v_exp_f32 v208, v208\n"
"v_exp_f32 v209, v209\n"
"v_smfmac_f32_32x32x32_bf16 v[192:207], a[192:195], v[232:239], v45\n"
"v_exp_f32 v210, v210\n"
"v_exp_f32 v211, v211\n"
"v_exp_f32 v212, v212\n"
"v_mfma_f32_32x32x16_bf16 v[224:239], a[196:199], v[132:135], 0\n"
"v_exp_f32 v213, v213\n"
"v_exp_f32 v214, v214\n"
"v_exp_f32 v215, v215\n"
"v_exp_f32 v216, v216\n"
"v_exp_f32 v217, v217\n"
"v_smfmac_f32_32x32x32_bf16 v[144:159], a[200:203], v[208:215], v45\n"
"v_exp_f32 v218, v218\n"
"v_exp_f32 v219, v219\n"
"v_exp_f32 v220, v220\n"
"v_exp_f32 v221, v221\n"
"v_exp_f32 v222, v222\n"
"v_exp_f32 v223, v223\n"
"v_exp_f32 v224, v224\n"
"v_exp_f32 v225, v225\n"
"v_smfmac_f32_32x32x32_bf16 v[144:159], a[204:207], v[216:223], v45\n"
"v_exp_f32 v226, v226\n"
"v_exp_f32 v227, v227\n"
"v_exp_f32 v228, v228\n"
"v_mfma_f32_32x32x16_bf16 v[208:223], a[196:199], v[136:139], 0\n"
"v_exp_f32 v229, v229\n"
"v_exp_f32 v230, v230\n"
"v_exp_f32 v231, v231\n"
"v_exp_f32 v232, v232\n"
"v_exp_f32 v233, v233\n"
"v_smfmac_f32_32x32x32_bf16 v[160:175], a[200:203], v[224:231], v45\n"
"v_exp_f32 v234, v234\n"
"v_exp_f32 v235, v235\n"
"v_exp_f32 v236, v236\n"
"v_exp_f32 v237, v237\n"
"v_exp_f32 v238, v238\n"
"v_exp_f32 v239, v239\n"
"v_exp_f32 v208, v208\n"
"v_exp_f32 v209, v209\n"
"v_smfmac_f32_32x32x32_bf16 v[160:175], a[204:207], v[232:239], v45\n"
"v_exp_f32 v210, v210\n"
"v_exp_f32 v211, v211\n"
"v_exp_f32 v212, v212\n"
"v_mfma_f32_32x32x16_bf16 v[224:239], a[196:199], v[140:143], 0\n"
"v_exp_f32 v213, v213\n"
"v_exp_f32 v214, v214\n"
"v_exp_f32 v215, v215\n"
"v_exp_f32 v216, v216\n"
"v_exp_f32 v217, v217\n"
"v_smfmac_f32_32x32x32_bf16 v[176:191], a[200:203], v[208:215], v45\n"
"buffer_load_dwordx4 a[160:163], v2, s[40:43], s0 offen offset:0 sc1\n"
"buffer_load_dwordx4 a[172:175], v2, s[40:43], s0 offen offset:2048 sc1\n"
"v_exp_f32 v218, v218\n"
"v_exp_f32 v219, v219\n"
"v_exp_f32 v220, v220\n"
"v_exp_f32 v221, v221\n"
"v_exp_f32 v222, v222\n"
"v_exp_f32 v223, v223\n"
"v_exp_f32 v224, v224\n"
"v_exp_f32 v225, v225\n"
"v_smfmac_f32_32x32x32_bf16 v[176:191], a[204:207], v[216:223], v45\n"
"s_waitcnt vmcnt(14)\n"
"v_exp_f32 v226, v226\n"
"v_exp_f32 v227, v227\n"
"v_exp_f32 v228, v228\n"
"v_mfma_f32_32x32x16_bf16 v[208:223], a[208:211], v[128:131], 0\n"
"v_exp_f32 v229, v229\n"
"v_exp_f32 v230, v230\n"
"v_exp_f32 v231, v231\n"
"v_exp_f32 v232, v232\n"
"v_exp_f32 v233, v233\n"
"v_smfmac_f32_32x32x32_bf16 v[192:207], a[200:203], v[224:231], v45\n"
"v_exp_f32 v234, v234\n"
"v_exp_f32 v235, v235\n"
"v_exp_f32 v236, v236\n"
"v_exp_f32 v237, v237\n"
"v_exp_f32 v238, v238\n"
"v_exp_f32 v239, v239\n"
"v_exp_f32 v208, v208\n"
"v_exp_f32 v209, v209\n"
"v_smfmac_f32_32x32x32_bf16 v[192:207], a[204:207], v[232:239], v45\n"
"v_exp_f32 v210, v210\n"
"v_exp_f32 v211, v211\n"
"v_exp_f32 v212, v212\n"
"v_mfma_f32_32x32x16_bf16 v[224:239], a[208:211], v[132:135], 0\n"
"s_xor_b32 s0, s0, 8192\n"
"v_exp_f32 v213, v213\n"
"v_exp_f32 v214, v214\n"
"v_exp_f32 v215, v215\n"
"v_exp_f32 v216, v216\n"
"v_exp_f32 v217, v217\n"
"v_smfmac_f32_32x32x32_bf16 v[144:159], a[212:215], v[208:215], v45\n"
"s_mov_b64 exec, s[76:77]\n"
"buffer_load_dwordx4 a[188:191], v47, s[40:43], s0 offen offset:1024 sc1\n"
"buffer_load_dwordx4 a[192:195], v47, s[40:43], s0 offen offset:1536 sc1\n"
"buffer_load_dwordx4 a[200:203], v47, s[40:43], s0 offen offset:3072 sc1\n"
"buffer_load_dwordx4 a[204:207], v47, s[40:43], s0 offen offset:3584 sc1\n"
"s_mov_b64 exec, -1\n"
"s_add_u32 s60, s34, 0\n"
"buffer_load_dwordx2 v[112:113], v254, s[28:31], s60 offen\n"
"v_exp_f32 v218, v218\n"
"v_exp_f32 v219, v219\n"
"v_exp_f32 v220, v220\n"
"v_exp_f32 v221, v221\n"
"v_exp_f32 v222, v222\n"
"v_exp_f32 v223, v223\n"
"v_exp_f32 v224, v224\n"
"v_exp_f32 v225, v225\n"
"v_smfmac_f32_32x32x32_bf16 v[144:159], a[216:219], v[216:223], v45\n"
"v_exp_f32 v226, v226\n"
"v_exp_f32 v227, v227\n"
"v_exp_f32 v228, v228\n"
"v_mfma_f32_32x32x16_bf16 v[208:223], a[208:211], v[136:139], 0\n"
"v_exp_f32 v229, v229\n"
"v_exp_f32 v230, v230\n"
"v_exp_f32 v231, v231\n"
"v_exp_f32 v232, v232\n"
"v_exp_f32 v233, v233\n"
"v_smfmac_f32_32x32x32_bf16 v[160:175], a[212:215], v[224:231], v45\n"
"s_add_u32 s60, s34, 16384\n"
"buffer_load_dwordx2 v[114:115], v254, s[28:31], s60 offen\n"
"v_exp_f32 v234, v234\n"
"v_exp_f32 v235, v235\n"
"v_exp_f32 v236, v236\n"
"v_exp_f32 v237, v237\n"
"v_exp_f32 v238, v238\n"
"v_exp_f32 v239, v239\n"
"v_exp_f32 v208, v208\n"
"v_exp_f32 v209, v209\n"
"v_smfmac_f32_32x32x32_bf16 v[160:175], a[216:219], v[232:239], v45\n"
"v_exp_f32 v210, v210\n"
"v_exp_f32 v211, v211\n"
"v_exp_f32 v212, v212\n"
"v_mfma_f32_32x32x16_bf16 v[224:239], a[208:211], v[140:143], 0\n"
"v_exp_f32 v213, v213\n"
"v_exp_f32 v214, v214\n"
"v_exp_f32 v215, v215\n"
"v_exp_f32 v216, v216\n"
"v_exp_f32 v217, v217\n"
"v_smfmac_f32_32x32x32_bf16 v[176:191], a[212:215], v[208:215], v45\n"
"s_add_u32 s60, s34, 32768\n"
"buffer_load_dwordx2 v[116:117], v254, s[28:31], s60 offen\n"
"v_exp_f32 v218, v218\n"
"v_exp_f32 v219, v219\n"
"v_exp_f32 v220, v220\n"
"v_exp_f32 v221, v221\n"
"v_exp_f32 v222, v222\n"
"v_exp_f32 v223, v223\n"
"v_exp_f32 v224, v224\n"
"v_exp_f32 v225, v225\n"
"v_smfmac_f32_32x32x32_bf16 v[176:191], a[216:219], v[216:223], v45\n"
"s_waitcnt vmcnt(20)\n"
"v_exp_f32 v226, v226\n"
"v_exp_f32 v227, v227\n"
"v_exp_f32 v228, v228\n"
"v_mfma_f32_32x32x16_bf16 v[208:223], a[220:223], v[128:131], 0\n"
"v_exp_f32 v229, v229\n"
"v_exp_f32 v230, v230\n"
"v_exp_f32 v231, v231\n"
"v_exp_f32 v232, v232\n"
"v_exp_f32 v233, v233\n"
"v_smfmac_f32_32x32x32_bf16 v[192:207], a[212:215], v[224:231], v45\n"
"s_add_u32 s60, s34, 49152\n"
"buffer_load_dwordx2 v[118:119], v254, s[28:31], s60 offen\n"
"v_exp_f32 v234, v234\n"
"v_exp_f32 v235, v235\n"
"v_exp_f32 v236, v236\n"
"v_exp_f32 v237, v237\n"
"v_exp_f32 v238, v238\n"
"v_exp_f32 v239, v239\n"
"v_exp_f32 v208, v208\n"
"v_exp_f32 v209, v209\n"
"v_smfmac_f32_32x32x32_bf16 v[192:207], a[216:219], v[232:239], v45\n"
"v_exp_f32 v210, v210\n"
"v_exp_f32 v211, v211\n"
"v_exp_f32 v212, v212\n"
"v_mfma_f32_32x32x16_bf16 v[224:239], a[220:223], v[132:135], 0\n"
"v_exp_f32 v213, v213\n"
"v_exp_f32 v214, v214\n"
"v_exp_f32 v215, v215\n"
"v_exp_f32 v216, v216\n"
"v_exp_f32 v217, v217\n"
"v_smfmac_f32_32x32x32_bf16 v[144:159], a[224:227], v[208:215], v45\n"
"buffer_load_dwordx4 a[184:187], v2, s[40:43], s0 offen offset:0 sc1\n"
"buffer_load_dwordx4 a[196:199], v2, s[40:43], s0 offen offset:2048 sc1\n"
"v_exp_f32 v218, v218\n"
"v_exp_f32 v219, v219\n"
"v_exp_f32 v220, v220\n"
"v_exp_f32 v221, v221\n"
"v_exp_f32 v222, v222\n"
"v_exp_f32 v223, v223\n"
"v_exp_f32 v224, v224\n"
"v_exp_f32 v225, v225\n"
"v_smfmac_f32_32x32x32_bf16 v[144:159], a[228:231], v[216:223], v45\n"
"v_exp_f32 v226, v226\n"
"v_exp_f32 v227, v227\n"
"v_exp_f32 v228, v228\n"
"v_mfma_f32_32x32x16_bf16 v[208:223], a[220:223], v[136:139], 0\n"
"v_exp_f32 v229, v229\n"
"v_exp_f32 v230, v230\n"
"v_exp_f32 v231, v231\n"
"v_exp_f32 v232, v232\n"
"v_exp_f32 v233, v233\n"
"v_smfmac_f32_32x32x32_bf16 v[160:175], a[224:227], v[224:231], v45\n"
"v_exp_f32 v234, v234\n"
"v_exp_f32 v235, v235\n"
"v_exp_f32 v236, v236\n"
"v_exp_f32 v237, v237\n"
"v_exp_f32 v238, v238\n"
"v_exp_f32 v239, v239\n"
"v_exp_f32 v208, v208\n"
"v_exp_f32 v209, v209\n"
"v_smfmac_f32_32x32x32_bf16 v[160:175], a[228:231], v[232:239], v45\n"
"v_exp_f32 v210, v210\n"
"v_exp_f32 v211, v211\n"
"v_exp_f32 v212, v212\n"
"v_mfma_f32_32x32x16_bf16 v[224:239], a[220:223], v[140:143], 0\n"
"v_exp_f32 v213, v213\n"
"v_exp_f32 v214, v214\n"
"v_exp_f32 v215, v215\n"
"v_exp_f32 v216, v216\n"
"v_exp_f32 v217, v217\n"
"v_smfmac_f32_32x32x32_bf16 v[176:191], a[224:227], v[208:215], v45\n"
"buffer_load_dwordx4 v[28:31], v17, s[40:43], 0 offen offset:128 sc1\n"
"v_exp_f32 v218, v218\n"
"v_exp_f32 v219, v219\n"
"v_exp_f32 v220, v220\n"
"v_exp_f32 v221, v221\n"
"v_exp_f32 v222, v222\n"
"v_exp_f32 v223, v223\n"
"v_exp_f32 v224, v224\n"
"v_exp_f32 v225, v225\n"
"v_smfmac_f32_32x32x32_bf16 v[176:191], a[228:231], v[216:223], v45\n"
"s_waitcnt vmcnt(18)\n"
"v_exp_f32 v226, v226\n"
"v_exp_f32 v227, v227\n"
"v_exp_f32 v228, v228\n"
"v_mfma_f32_32x32x16_bf16 v[208:223], a[232:235], v[128:131], 0\n"
"v_exp_f32 v229, v229\n"
"v_exp_f32 v230, v230\n"
"v_exp_f32 v231, v231\n"
"v_exp_f32 v232, v232\n"
"v_exp_f32 v233, v233\n"
"v_smfmac_f32_32x32x32_bf16 v[192:207], a[224:227], v[224:231], v45\n"
"v_exp_f32 v234, v234\n"
"v_exp_f32 v235, v235\n"
"v_exp_f32 v236, v236\n"
"v_exp_f32 v237, v237\n"
"v_exp_f32 v238, v238\n"
"v_exp_f32 v239, v239\n"
"v_exp_f32 v208, v208\n"
"v_exp_f32 v209, v209\n"
"v_smfmac_f32_32x32x32_bf16 v[192:207], a[228:231], v[232:239], v45\n"
"v_exp_f32 v210, v210\n"
"v_exp_f32 v211, v211\n"
"v_exp_f32 v212, v212\n"
"v_mfma_f32_32x32x16_bf16 v[224:239], a[232:235], v[132:135], 0\n"
"s_xor_b32 s0, s0, 16384\n"
"v_exp_f32 v213, v213\n"
"v_exp_f32 v214, v214\n"
"v_exp_f32 v215, v215\n"
"v_exp_f32 v216, v216\n"
"v_exp_f32 v217, v217\n"
"v_smfmac_f32_32x32x32_bf16 v[144:159], a[236:239], v[208:215], v45\n"
"s_mov_b64 exec, s[76:77]\n"
"buffer_load_dwordx4 a[212:215], v47, s[40:43], s0 offen offset:1024 sc1\n"
"buffer_load_dwordx4 a[216:219], v47, s[40:43], s0 offen offset:1536 sc1\n"
"buffer_load_dwordx4 a[224:227], v47, s[40:43], s0 offen offset:3072 sc1\n"
"buffer_load_dwordx4 a[228:231], v47, s[40:43], s0 offen offset:3584 sc1\n"
"s_mov_b64 exec, -1\n"
"v_exp_f32 v218, v218\n"
"v_exp_f32 v219, v219\n"
"v_exp_f32 v220, v220\n"
"v_exp_f32 v221, v221\n"
"v_exp_f32 v222, v222\n"
"v_exp_f32 v223, v223\n"
"v_exp_f32 v224, v224\n"
"v_exp_f32 v225, v225\n"
"v_smfmac_f32_32x32x32_bf16 v[144:159], a[240:243], v[216:223], v45\n"
"v_exp_f32 v226, v226\n"
"v_exp_f32 v227, v227\n"
"v_exp_f32 v228, v228\n"
"v_mfma_f32_32x32x16_bf16 v[208:223], a[232:235], v[136:139], 0\n"
"v_exp_f32 v229, v229\n"
"v_exp_f32 v230, v230\n"
"v_exp_f32 v231, v231\n"
"v_exp_f32 v232, v232\n"
"v_exp_f32 v233, v233\n"
"v_smfmac_f32_32x32x32_bf16 v[160:175], a[236:239], v[224:231], v45\n"
"v_exp_f32 v234, v234\n"
"v_exp_f32 v235, v235\n"
"v_exp_f32 v236, v236\n"
"v_exp_f32 v237, v237\n"
"v_exp_f32 v238, v238\n"
"v_exp_f32 v239, v239\n"
"v_exp_f32 v208, v208\n"
"v_exp_f32 v209, v209\n"
"v_smfmac_f32_32x32x32_bf16 v[160:175], a[240:243], v[232:239], v45\n"
"v_exp_f32 v210, v210\n"
"v_exp_f32 v211, v211\n"
"v_exp_f32 v212, v212\n"
"v_mfma_f32_32x32x16_bf16 v[224:239], a[232:235], v[140:143], 0\n"
"v_exp_f32 v213, v213\n"
"v_exp_f32 v214, v214\n"
"v_exp_f32 v215, v215\n"
"v_exp_f32 v216, v216\n"
"v_exp_f32 v217, v217\n"
"v_smfmac_f32_32x32x32_bf16 v[176:191], a[236:239], v[208:215], v45\n"
"v_exp_f32 v218, v218\n"
"v_exp_f32 v219, v219\n"
"v_exp_f32 v220, v220\n"
"v_exp_f32 v221, v221\n"
"v_exp_f32 v222, v222\n"
"v_exp_f32 v223, v223\n"
"v_exp_f32 v224, v224\n"
"v_exp_f32 v225, v225\n"
"v_smfmac_f32_32x32x32_bf16 v[176:191], a[240:243], v[216:223], v45\n"
"s_waitcnt vmcnt(21)\n"
"v_exp_f32 v226, v226\n"
"v_exp_f32 v227, v227\n"
"v_exp_f32 v228, v228\n"
"v_mfma_f32_32x32x16_bf16 v[208:223], a[244:247], v[128:131], 0\n"
"v_exp_f32 v229, v229\n"
"v_exp_f32 v230, v230\n"
"v_exp_f32 v231, v231\n"
"v_exp_f32 v232, v232\n"
"v_exp_f32 v233, v233\n"
"v_smfmac_f32_32x32x32_bf16 v[192:207], a[236:239], v[224:231], v45\n"
"v_exp_f32 v234, v234\n"
"v_exp_f32 v235, v235\n"
"v_exp_f32 v236, v236\n"
"v_exp_f32 v237, v237\n"
"v_exp_f32 v238, v238\n"
"v_exp_f32 v239, v239\n"
"v_exp_f32 v208, v208\n"
"v_exp_f32 v209, v209\n"
"v_smfmac_f32_32x32x32_bf16 v[192:207], a[240:243], v[232:239], v45\n"
"v_exp_f32 v210, v210\n"
"v_exp_f32 v211, v211\n"
"v_exp_f32 v212, v212\n"
"v_mfma_f32_32x32x16_bf16 v[224:239], a[244:247], v[132:135], 0\n"
"v_exp_f32 v213, v213\n"
"v_exp_f32 v214, v214\n"
"v_exp_f32 v215, v215\n"
"v_exp_f32 v216, v216\n"
"v_exp_f32 v217, v217\n"
"v_smfmac_f32_32x32x32_bf16 v[144:159], a[248:251], v[208:215], v45\n"
"buffer_load_dwordx4 a[208:211], v2, s[40:43], s0 offen offset:0 sc1\n"
"buffer_load_dwordx4 a[220:223], v2, s[40:43], s0 offen offset:2048 sc1\n"
"v_exp_f32 v218, v218\n"
"v_exp_f32 v219, v219\n"
"v_exp_f32 v220, v220\n"
"v_exp_f32 v221, v221\n"
"v_exp_f32 v222, v222\n"
"v_exp_f32 v223, v223\n"
"v_exp_f32 v224, v224\n"
"v_exp_f32 v225, v225\n"
"v_smfmac_f32_32x32x32_bf16 v[144:159], a[252:255], v[216:223], v45\n"
"v_exp_f32 v226, v226\n"
"v_exp_f32 v227, v227\n"
"v_exp_f32 v228, v228\n"
"v_mfma_f32_32x32x16_bf16 v[208:223], a[244:247], v[136:139], 0\n"
"s_xor_b32 s0, s0, 8192\n"
"v_exp_f32 v229, v229\n"
"v_exp_f32 v230, v230\n"
"v_exp_f32 v231, v231\n"
"v_exp_f32 v232, v232\n"
"v_exp_f32 v233, v233\n"
"v_smfmac_f32_32x32x32_bf16 v[160:175], a[248:251], v[224:231], v45\n"
"s_mov_b64 exec, s[76:77]\n"
"buffer_load_dwordx4 a[236:239], v47, s[40:43], s0 offen offset:1024 sc1\n"
"buffer_load_dwordx4 a[240:243], v47, s[40:43], s0 offen offset:1536 sc1\n"
"buffer_load_dwordx4 a[152:155], v47, s[40:43], s0 offen offset:3072 sc1\n"
"buffer_load_dwordx4 a[156:159], v47, s[40:43], s0 offen offset:3584 sc1\n"
"s_mov_b64 exec, -1\n"
"v_exp_f32 v234, v234\n"
"v_exp_f32 v235, v235\n"
"v_exp_f32 v236, v236\n"
"v_exp_f32 v237, v237\n"
"v_exp_f32 v238, v238\n"
"v_exp_f32 v239, v239\n"
"v_exp_f32 v208, v208\n"
"v_exp_f32 v209, v209\n"
"v_smfmac_f32_32x32x32_bf16 v[160:175], a[252:255], v[232:239], v45\n"
"v_exp_f32 v210, v210\n"
"v_exp_f32 v211, v211\n"
"v_exp_f32 v212, v212\n"
"v_mfma_f32_32x32x16_bf16 v[224:239], a[244:247], v[140:143], 0\n"
"v_exp_f32 v213, v213\n"
"v_exp_f32 v214, v214\n"
"v_exp_f32 v215, v215\n"
"v_exp_f32 v216, v216\n"
"v_exp_f32 v217, v217\n"
"v_smfmac_f32_32x32x32_bf16 v[176:191], a[248:251], v[208:215], v45\n"
"v_exp_f32 v218, v218\n"
"v_exp_f32 v219, v219\n"
"v_exp_f32 v220, v220\n"
"v_exp_f32 v221, v221\n"
"v_exp_f32 v222, v222\n"
"v_exp_f32 v223, v223\n"
"v_exp_f32 v224, v224\n"
"v_exp_f32 v225, v225\n"
"v_smfmac_f32_32x32x32_bf16 v[176:191], a[252:255], v[216:223], v45\n"
"s_waitcnt vmcnt(22)\n"
"s_waitcnt vmcnt(10)\n"
"v_cmp_ne_u64 vcc, s[58:59], v[28:29]\n"
"s_cbranch_vccnz L_tslow\n"
"v_cmp_ne_u64 vcc, s[58:59], v[30:31]\n"
"s_cbranch_vccnz L_tslow\n"
"L_tback:\n"
"v_exp_f32 v226, v226\n"
"v_exp_f32 v227, v227\n"
"v_exp_f32 v228, v228\n"
"v_mfma_f32_32x32x16_bf16 v[208:223], a[160:163], v[128:131], 0\n"
"v_exp_f32 v229, v229\n"
"v_exp_f32 v230, v230\n"
"v_exp_f32 v231, v231\n"
"v_exp_f32 v232, v232\n"
"v_exp_f32 v233, v233\n"
"v_smfmac_f32_32x32x32_bf16 v[192:207], a[248:251], v[224:231], v45\n"
"v_exp_f32 v234, v234\n"
"v_exp_f32 v235, v235\n"
"v_exp_f32 v236, v236\n"
"v_exp_f32 v237, v237\n"
"v_exp_f32 v238, v238\n"
"v_exp_f32 v239, v239\n"
"v_exp_f32 v208, v208\n"
"v_exp_f32 v209, v209\n"
"v_smfmac_f32_32x32x32_bf16 v[192:207], a[252:255], v[232:239], v45\n"
"v_exp_f32 v210, v210\n"
"v_exp_f32 v211, v211\n"
"v_exp_f32 v212, v212\n"
"v_mfma_f32_32x32x16_bf16 v[224:239], a[160:163], v[132:135], 0\n"
"v_exp_f32 v213, v213\n"
"v_exp_f32 v214, v214\n"
"v_exp_f32 v215, v215\n"
"v_exp_f32 v216, v216\n"
"v_exp_f32 v217, v217\n"
"v_smfmac_f32_32x32x32_bf16 v[144:159], a[164:167], v[208:215], v45\n"
"v_exp_f32 v218, v218\n"
"v_exp_f32 v219, v219\n"
"v_exp_f32 v220, v220\n"
"v_exp_f32 v221, v221\n"
"v_exp_f32 v222, v222\n"
"v_exp_f32 v223, v223\n"
"v_exp_f32 v224, v224\n"
"v_exp_f32 v225, v225\n"
"v_smfmac_f32_32x32x32_bf16 v[144:159], a[168:171], v[216:223], v45\n"
"v_exp_f32 v226, v226\n"
"v_exp_f32 v227, v227\n"
"v_exp_f32 v228, v228\n"
"v_mfma_f32_32x32x16_bf16 v[208:223], a[160:163], v[136:139], 0\n"
"v_exp_f32 v229, v229\n"
"v_exp_f32 v230, v230\n"
"v_exp_f32 v231, v231\n"
"v_exp_f32 v232, v232\n"
"v_exp_f32 v233, v233\n"
"v_smfmac_f32_32x32x32_bf16 v[160:175], a[164:167], v[224:231], v45\n"
"buffer_load_dwordx4 a[232:235], v2, s[40:43], s0 offen offset:0 sc1\n"
"buffer_load_dwordx4 a[148:151], v2, s[40:43], s0 offen offset:2048 sc1\n"
"v_exp_f32 v234, v234\n"
"v_exp_f32 v235, v235\n"
"v_exp_f32 v236, v236\n"
"v_exp_f32 v237, v237\n"
"v_exp_f32 v238, v238\n"
"v_exp_f32 v239, v239\n"
"v_exp_f32 v208, v208\n"
"v_exp_f32 v209, v209\n"
"v_smfmac_f32_32x32x32_bf16 v[160:175], a[168:171], v[232:239], v45\n"
"s_add_u32 m0, s2, 4096\n"
"s_add_u32 s1, s1, 4096\n"
"v_exp_f32 v210, v210\n"
"v_exp_f32 v211, v211\n"
"v_exp_f32 v212, v212\n"
"v_mfma_f32_32x32x16_bf16 v[224:239], a[160:163], v[140:143], 0\n"
"v_exp_f32 v213, v213\n"
"v_exp_f32 v214, v214\n"
"v_exp_f32 v215, v215\n"
"v_exp_f32 v216, v216\n"
"v_exp_f32 v217, v217\n"
"v_smfmac_f32_32x32x32_bf16 v[176:191], a[164:167], v[208:215], v45\n"
"buffer_load_dwordx4 v2, s[40:43], s1 offen offset:0 sc1 lds\n"
"buffer_load_dwordx4 v2, s[40:43], s1 offen offset:1024 sc1 lds\n"
"v_exp_f32 v218, v218\n"
"v_exp_f32 v219, v219\n"
"v_exp_f32 v220, v220\n"
"v_exp_f32 v221, v221\n"
"v_exp_f32 v222, v222\n"
"v_exp_f32 v223, v223\n"
"v_exp_f32 v224, v224\n"
"v_exp_f32 v225, v225\n"
"v_smfmac_f32_32x32x32_bf16 v[176:191], a[168:171], v[216:223], v45\n"
"v_exp_f32 v226, v226\n"
"v_exp_f32 v227, v227\n"
"v_exp_f32 v228, v228\n"
"v_mfma_f32_32x32x16_bf16 v[208:223], a[172:175], v[128:131], 0\n"
"v_exp_f32 v229, v229\n"
"v_exp_f32 v230, v230\n"
"v_exp_f32 v231, v231\n"
"v_exp_f32 v232, v232\n"
"v_exp_f32 v233, v233\n"
"v_smfmac_f32_32x32x32_bf16 v[192:207], a[164:167], v[224:231], v45\n"
"buffer_load_dwordx4 v2, s[40:43], s1 offen offset:2048 sc1 lds\n"
"buffer_load_dwordx4 v2, s[40:43], s1 offen offset:3072 sc1 lds\n"
"v_exp_f32 v234, v234\n"
"v_exp_f32 v235, v235\n"
"v_exp_f32 v236, v236\n"
"v_exp_f32 v237, v237\n"
"v_exp_f32 v238, v238\n"
"v_exp_f32 v239, v239\n"
"v_exp_f32 v208, v208\n"
"v_exp_f32 v209, v209\n"
"v_smfmac_f32_32x32x32_bf16 v[192:207], a[168:171], v[232:239], v45\n"
"v_exp_f32 v210, v210\n"
"v_exp_f32 v211, v211\n"
"v_exp_f32 v212, v212\n"
"v_mfma_f32_32x32x16_bf16 v[224:239], a[172:175], v[132:135], 0\n"
"v_exp_f32 v213, v213\n"
"v_exp_f32 v214, v214\n"
"v_exp_f32 v215, v215\n"
"v_exp_f32 v216, v216\n"
"v_exp_f32 v217, v217\n"
"v_smfmac_f32_32x32x32_bf16 v[144:159], a[176:179], v[208:215], v45\n"
"v_exp_f32 v218, v218\n"
"v_exp_f32 v219, v219\n"
"v_exp_f32 v220, v220\n"
"v_exp_f32 v221, v221\n"
"v_exp_f32 v222, v222\n"
"v_exp_f32 v223, v223\n"
"v_exp_f32 v224, v224\n"
"v_exp_f32 v225, v225\n"
"v_smfmac_f32_32x32x32_bf16 v[144:159], a[180:183], v[216:223], v45\n"
"s_add_u32 m0, s2, 8192\n"
"s_add_u32 s1, s1, 4096\n"
"v_exp_f32 v226, v226\n"
"v_exp_f32 v227, v227\n"
"v_exp_f32 v228, v228\n"
"v_mfma_f32_32x32x16_bf16 v[208:223], a[172:175], v[136:139], 0\n"
"v_exp_f32 v229, v229\n"
"v_exp_f32 v230, v230\n"
"v_exp_f32 v231, v231\n"
"v_exp_f32 v232, v232\n"
"v_exp_f32 v233, v233\n"
"v_smfmac_f32_32x32x32_bf16 v[160:175], a[176:179], v[224:231], v45\n"
"buffer_load_dwordx4 v2, s[40:43], s1 offen offset:0 sc1 lds\n"
"buffer_load_dwordx4 v2, s[40:43], s1 offen offset:1024 sc1 lds\n"
"v_exp_f32 v234, v234\n"
"v_exp_f32 v235, v235\n"
"v_exp_f32 v236, v236\n"
"v_exp_f32 v237, v237\n"
"v_exp_f32 v238, v238\n"
"v_exp_f32 v239, v239\n"
"v_exp_f32 v208, v208\n"
"v_exp_f32 v209, v209\n"
"v_smfmac_f32_32x32x32_bf16 v[160:175], a[180:183], v[232:239], v45\n"
"v_exp_f32 v210, v210\n"
"v_exp_f32 v211, v211\n"
"v_exp_f32 v212, v212\n"
"v_mfma_f32_32x32x16_bf16 v[224:239], a[172:175], v[140:143], 0\n"
"v_exp_f32 v213, v213\n"
"v_exp_f32 v214, v214\n"
"v_exp_f32 v215, v215\n"
"v_exp_f32 v216, v216\n"
"v_exp_f32 v217, v217\n"
"v_smfmac_f32_32x32x32_bf16 v[176:191], a[176:179], v[208:215], v45\n"
"buffer_load_dwordx4 v2, s[40:43], s1 offen offset:2048 sc1 lds\n"
"buffer_load_dwordx4 v2, s[40:43], s1 offen offset:3072 sc1 lds\n"
"v_exp_f32 v218, v218\n"
"v_exp_f32 v219, v219\n"
"v_exp_f32 v220, v220\n"
"v_exp_f32 v221, v221\n"
"v_exp_f32 v222, v222\n"
"v_exp_f32 v223, v223\n"
"v_exp_f32 v224, v224\n"
"v_exp_f32 v225, v225\n"
"v_smfmac_f32_32x32x32_bf16 v[176:191], a[180:183], v[216:223], v45\n"
"L_pad6:\n"
"s_cbranch_execz L_ret10\n"
"s_add_u32 m0, s2, 12288\n"
"s_add_u32 s1, s1, 4096\n"
"v_exp_f32 v226, v226\n"
"v_exp_f32 v227, v227\n"
"v_exp_f32 v228, v228\n"
"v_mfma_f32_32x32x16_bf16 v[208:223], a[184:187], v[128:131], 0\n"
"v_exp_f32 v229, v229\n"
"v_exp_f32 v230, v230\n"
"v_exp_f32 v231, v231\n"
"v_exp_f32 v232, v232\n"
"v_exp_f32 v233, v233\n"
"v_smfmac_f32_32x32x32_bf16 v[192:207], a[176:179], v[224:231], v45\n"
"buffer_load_dwordx4 v2, s[40:43], s1 offen offset:0 sc1 lds\n"
"buffer_load_dwordx4 v2, s[40:43], s1 offen offset:1024 sc1 lds\n"
"v_exp_f32 v234, v234\n"
"v_exp_f32 v235, v235\n"
"v_exp_f32 v236, v236\n"
"v_exp_f32 v237, v237\n"
"v_exp_f32 v238, v238\n"
"v_exp_f32 v239, v239\n"
"v_exp_f32 v208, v208\n"
"v_exp_f32 v209, v209\n"
"v_smfmac_f32_32x32x32_bf16 v[192:207], a[180:183], v[232:239], v45\n"
"v_exp_f32 v210, v210\n"
"v_exp_f32 v211, v211\n"
"v_exp_f32 v212, v212\n"
"v_mfma_f32_32x32x16_bf16 v[224:239], a[184:187], v[132:135], 0\n"
"s_xor_b32 s0, s0, 32768\n"
"v_exp_f32 v213, v213\n"
"v_exp_f32 v214, v214\n"
"v_exp_f32 v215, v215\n"
"v_exp_f32 v216, v216\n"
"v_exp_f32 v217, v217\n"
"v_smfmac_f32_32x32x32_bf16 v[144:159], a[188:191], v[208:215], v45\n"
"s_mov_b64 exec, s[76:77]\n"
"buffer_load_dwordx4 a[164:167], v47, s[40:43], s0 offen offset:1024 sc1\n"
"buffer_load_dwordx4 a[168:171], v47, s[40:43], s0 offen offset:1536 sc1\n"
"buffer_load_dwordx4 a[176:179], v47, s[40:43], s0 offen offset:3072 sc1\n"
"buffer_load_dwordx4 a[180:183], v47, s[40:43], s0 offen offset:3584 sc1\n"
"s_mov_b64 exec, -1\n"
"v_exp_f32 v218, v218\n"
"v_exp_f32 v219, v219\n"
"v_exp_f32 v220, v220\n"
"v_exp_f32 v221, v221\n"
"v_exp_f32 v222, v222\n"
"v_exp_f32 v223, v223\n"
"v_exp_f32 v224, v224\n"
"v_exp_f32 v225, v225\n"
"v_smfmac_f32_32x32x32_bf16 v[144:159], a[192:195], v[216:223], v45\n"
"v_exp_f32 v226, v226\n"
"v_exp_f32 v227, v227\n"
"v_exp_f32 v228, v228\n"
"v_mfma_f32_32x32x16_bf16 v[208:223], a[184:187], v[136:139], 0\n"
"v_exp_f32 v229, v229\n"
"v_exp_f32 v230, v230\n"
"v_exp_f32 v231, v231\n"
"v_exp_f32 v232, v232\n"
"v_exp_f32 v233, v233\n"
"v_smfmac_f32_32x32x32_bf16 v[160:175], a[188:191], v[224:231], v45\n"
"buffer_load_dwordx4 v2, s[40:43], s1 offen offset:2048 sc1 lds\n"
"buffer_load_dwordx4 v2, s[40:43], s1 offen offset:3072 sc1 lds\n"
"v_exp_f32 v234, v234\n"
"v_exp_f32 v235, v235\n"
"v_exp_f32 v236, v236\n"
"v_exp_f32 v237, v237\n"
"v_exp_f32 v238, v238\n"
"v_exp_f32 v239, v239\n"
"v_exp_f32 v208, v208\n"
"v_exp_f32 v209, v209\n"
"v_smfmac_f32_32x32x32_bf16 v[160:175], a[192:195], v[232:239], v45\n"
"s_add_u32 m0, s2, 16384\n"
"s_add_u32 s1, s1, 4096\n"
"v_exp_f32 v210, v210\n"
"v_exp_f32 v211, v211\n"
"v_exp_f32 v212, v212\n"
"v_mfma_f32_32x32x16_bf16 v[224:239], a[184:187], v[140:143], 0\n"
"v_exp_f32 v213, v213\n"
"v_exp_f32 v214, v214\n"
"v_exp_f32 v215, v215\n"
"v_exp_f32 v216, v216\n"
"v_exp_f32 v217, v217\n"
"v_smfmac_f32_32x32x32_bf16 v[176:191], a[188:191], v[208:215], v45\n"
"buffer_load_dwordx4 v2, s[40:43], s1 offen offset:0 sc1 lds\n"
"buffer_load_dwordx4 v2, s[40:43], s1 offen offset:1024 sc1 lds\n"
"v_exp_f32 v218, v218\n"
"v_exp_f32 v219, v219\n"
"v_exp_f32 v220, v220\n"
"v_exp_f32 v221, v221\n"
"v_exp_f32 v222, v222\n"
"v_exp_f32 v223, v223\n"
"v_exp_f32 v224, v224\n"
"v_exp_f32 v225, v225\n"
"v_smfmac_f32_32x32x32_bf16 v[176:191], a[192:195], v[216:223], v45\n"
"v_exp_f32 v226, v226\n"
"v_exp_f32 v227, v227\n"
"v_exp_f32 v228, v228\n"
"v_mfma_f32_32x32x16_bf16 v[208:223], a[196:199], v[128:131], 0\n"
"v_exp_f32 v229, v229\n"
"v_exp_f32 v230, v230\n"
"v_exp_f32 v231, v231\n"
"v_exp_f32 v232, v232\n"
"v_exp_f32 v233, v233\n"
"v_smfmac_f32_32x32x32_bf16 v[192:207], a[188:191], v[224:231], v45\n"
"buffer_load_dwordx4 v2, s[40:43], s1 offen offset:2048 sc1 lds\n"
"buffer_load_dwordx4 v2, s[40:43], s1 offen offset:3072 sc1 lds\n"
"v_exp_f32 v234, v234\n"
"v_exp_f32 v235, v235\n"
"v_exp_f32 v236, v236\n"
"v_exp_f32 v237, v237\n"
"v_exp_f32 v238, v238\n"
"v_exp_f32 v239, v239\n"
"v_exp_f32 v208, v208\n"
"v_exp_f32 v209, v209\n"
"v_smfmac_f32_32x32x32_bf16 v[192:207], a[192:195], v[232:239], v45\n"
"s_add_u32 m0, s2, 20480\n"
"s_add_u32 s1, s1, 4096\n"
"v_exp_f32 v210, v210\n"
"v_exp_f32 v211, v211\n"
"v_exp_f32 v212, v212\n"
"v_mfma_f32_32x32x16_bf16 v[224:239], a[196:199], v[132:135], 0\n"
"v_exp_f32 v213, v213\n"
"v_exp_f32 v214, v214\n"
"v_exp_f32 v215, v215\n"
"v_exp_f32 v216, v216\n"
"v_exp_f32 v217, v217\n"
"v_smfmac_f32_32x32x32_bf16 v[144:159], a[200:203], v[208:215], v45\n"
"buffer_load_dwordx4 a[160:163], v2, s[40:43], s0 offen offset:0 sc1\n"
"buffer_load_dwordx4 a[172:175], v2, s[40:43], s0 offen offset:2048 sc1\n"
"buffer_load_dwordx4 v2, s[40:43], s1 offen offset:0 sc1 lds\n"
"buffer_load_dwordx4 v2, s[40:43], s1 offen offset:1024 sc1 lds\n"
"v_exp_f32 v218, v218\n"
"v_exp_f32 v219, v219\n"
"v_exp_f32 v220, v220\n"
"v_exp_f32 v221, v221\n"
"v_exp_f32 v222, v222\n"
"v_exp_f32 v223, v223\n"
"v_exp_f32 v224, v224\n"
"v_exp_f32 v225, v225\n"
"v_smfmac_f32_32x32x32_bf16 v[144:159], a[204:207], v[216:223], v45\n"
"v_exp_f32 v226, v226\n"
"v_exp_f32 v227, v227\n"
"v_exp_f32 v228, v228\n"
"v_mfma_f32_32x32x16_bf16 v[208:223], a[196:199], v[136:139], 0\n"
"v_exp_f32 v229, v229\n"
"v_exp_f32 v230, v230\n"
"v_exp_f32 v231, v231\n"
"v_exp_f32 v232, v232\n"
"v_exp_f32 v233, v233\n"
"v_smfmac_f32_32x32x32_bf16 v[160:175], a[200:203], v[224:231], v45\n"
"buffer_load_dwordx4 v2, s[40:43], s1 offen offset:2048 sc1 lds\n"
"buffer_load_dwordx4 v2, s[40:43], s1 offen offset:3072 sc1 lds\n"
"v_exp_f32 v234, v234\n"
"v_exp_f32 v235, v235\n"
"v_exp_f32 v236, v236\n"
"v_exp_f32 v237, v237\n"
"v_exp_f32 v238, v238\n"
"v_exp_f32 v239, v239\n"
"v_exp_f32 v208, v208\n"
"v_exp_f32 v209, v209\n"
"v_smfmac_f32_32x32x32_bf16 v[160:175], a[204:207], v[232:239], v45\n"
"s_add_u32 m0, s2, 24576\n"
"s_add_u32 s1, s1, 4096\n"
"v_exp_f32 v210, v210\n"
"v_exp_f32 v211, v211\n"
"v_exp_f32 v212, v212\n"
"v_mfma_f32_32x32x16_bf16 v[224:239], a[196:199], v[140:143], 0\n"
"v_exp_f32 v213, v213\n"
"v_exp_f32 v214, v214\n"
"v_exp_f32 v215, v215\n"
"v_exp_f32 v216, v216\n"
"v_exp_f32 v217, v217\n"
"v_smfmac_f32_32x32x32_bf16 v[176:191], a[200:203], v[208:215], v45\n"
"buffer_load_dwordx4 v2, s[40:43], s1 offen offset:0 sc1 lds\n"
"buffer_load_dwordx4 v2, s[40:43], s1 offen offset:1024 sc1 lds\n"
"v_exp_f32 v218, v218\n"
"v_exp_f32 v219, v219\n"
"v_exp_f32 v220, v220\n"
"v_exp_f32 v221, v221\n"
"v_exp_f32 v222, v222\n"
"v_exp_f32 v223, v223\n"
"v_exp_f32 v224, v224\n"
"v_exp_f32 v225, v225\n"
"v_smfmac_f32_32x32x32_bf16 v[176:191], a[204:207], v[216:223], v45\n"
"s_waitcnt vmcnt(35)\n"
"v_exp_f32 v226, v226\n"
"v_exp_f32 v227, v227\n"
"v_exp_f32 v228, v228\n"
"v_mfma_f32_32x32x16_bf16 v[208:223], a[208:211], v[128:131], 0\n"
"v_exp_f32 v229, v229\n"
"v_exp_f32 v230, v230\n"
"v_exp_f32 v231, v231\n"
"v_exp_f32 v232, v232\n"
"v_exp_f32 v233, v233\n"
"v_smfmac_f32_32x32x32_bf16 v[192:207], a[200:203], v[224:231], v45\n"
"buffer_load_dwordx4 v2, s[40:43], s1 offen offset:2048 sc1 lds\n"
"buffer_load_dwordx4 v2, s[40:43], s1 offen offset:3072 sc1 lds\n"
"v_exp_f32 v234, v234\n"
"v_exp_f32 v235, v235\n"
"v_exp_f32 v236, v236\n"
"v_exp_f32 v237, v237\n"
"v_exp_f32 v238, v238\n"
"v_exp_f32 v239, v239\n"
"v_exp_f32 v208, v208\n"
"v_exp_f32 v209, v209\n"
"v_smfmac_f32_32x32x32_bf16 v[192:207], a[204:207], v[232:239], v45\n"
"v_exp_f32 v210, v210\n"
"v_exp_f32 v211, v211\n"
"v_exp_f32 v212, v212\n"
"v_mfma_f32_32x32x16_bf16 v[224:239], a[208:211], v[132:135], 0\n"
"s_xor_b32 s0, s0, 8192\n"
"v_exp_f32 v213, v213\n"
"v_exp_f32 v214, v214\n"
"v_exp_f32 v215, v215\n"
"v_exp_f32 v216, v216\n"
"v_exp_f32 v217, v217\n"
"v_smfmac_f32_32x32x32_bf16 v[144:159], a[212:215], v[208:215], v45\n"
"s_mov_b64 exec, s[76:77]\n"
"buffer_load_dwordx4 a[188:191], v47, s[40:43], s0 offen offset:1024 sc1\n"
"buffer_load_dwordx4 a[192:195], v47, s[40:43], s0 offen offset:1536 sc1\n"
"buffer_load_dwordx4 a[200:203], v47, s[40:43], s0 offen offset:3072 sc1\n"
"buffer_load_dwordx4 a[204:207], v47, s[40:43], s0 offen offset:3584 sc1\n"
"s_mov_b64 exec, -1\n"
"v_exp_f32 v218, v218\n"
"v_exp_f32 v219, v219\n"
"v_exp_f32 v220, v220\n"
"v_exp_f32 v221, v221\n"
"v_exp_f32 v222, v222\n"
"v_exp_f32 v223, v223\n"
"v_exp_f32 v224, v224\n"
"v_exp_f32 v225, v225\n"
"v_smfmac_f32_32x32x32_bf16 v[144:159], a[216:219], v[216:223], v45\n"
"s_add_u32 m0, s2, 28672\n"
"s_add_u32 s1, s1, 4096\n"
"v_exp_f32 v226, v226\n"
"v_exp_f32 v227, v227\n"
"v_exp_f32 v228, v228\n"
"v_mfma_f32_32x32x16_bf16 v[208:223], a[208:211], v[136:139], 0\n"
"v_exp_f32 v229, v229\n"
"v_exp_f32 v230, v230\n"
"v_exp_f32 v231, v231\n"
"v_exp_f32 v232, v232\n"
"v_exp_f32 v233, v233\n"
"v_smfmac_f32_32x32x32_bf16 v[160:175], a[212:215], v[224:231], v45\n"
"buffer_load_dwordx4 v2, s[40:43], s1 offen offset:0 sc1 lds\n"
"buffer_load_dwordx4 v2, s[40:43], s1 offen offset:1024 sc1 lds\n"
"v_exp_f32 v234, v234\n"
"v_exp_f32 v235, v235\n"
"v_exp_f32 v236, v236\n"
"v_exp_f32 v237, v237\n"
"v_exp_f32 v238, v238\n"
"v_exp_f32 v239, v239\n"
"v_exp_f32 v208, v208\n"
"v_exp_f32 v209, v209\n"
"v_smfmac_f32_32x32x32_bf16 v[160:175], a[216:219], v[232:239], v45\n"
"v_exp_f32 v210, v210\n"
"v_exp_f32 v211, v211\n"
"v_exp_f32 v212, v212\n"
"v_mfma_f32_32x32x16_bf16 v[224:239], a[208:211], v[140:143], 0\n"
"v_exp_f32 v213, v213\n"
"v_exp_f32 v214, v214\n"
"v_exp_f32 v215, v215\n"
"v_exp_f32 v216, v216\n"
"v_exp_f32 v217, v217\n"
"v_smfmac_f32_32x32x32_bf16 v[176:191], a[212:215], v[208:215], v45\n"
"buffer_load_dwordx4 v2, s[40:43], s1 offen offset:2048 sc1 lds\n"
"buffer_load_dwordx4 v2, s[40:43], s1 offen offset:3072 sc1 lds\n"
"v_exp_f32 v218, v218\n"
"v_exp_f32 v219, v219\n"
"v_exp_f32 v220, v220\n"
"v_exp_f32 v221, v221\n"
"v_exp_f32 v222, v222\n"
"v_exp_f32 v223, v223\n"
"v_exp_f32 v224, v224\n"
"v_exp_f32 v225, v225\n"
"v_smfmac_f32_32x32x32_bf16 v[176:191], a[216:219], v[216:223], v45\n"
"s_waitcnt vmcnt(44)\n"
"v_exp_f32 v226, v226\n"
"v_exp_f32 v227, v227\n"
"v_exp_f32 v228, v228\n"
"v_mfma_f32_32x32x16_bf16 v[208:223], a[220:223], v[128:131], 0\n"
"v_exp_f32 v229, v229\n"
"v_exp_f32 v230, v230\n"
"v_exp_f32 v231, v231\n"
"v_exp_f32 v232, v232\n"
"v_exp_f32 v233, v233\n"
"v_smfmac_f32_32x32x32_bf16 v[192:207], a[212:215], v[224:231], v45\n"
"buffer_load_dwordx4 v[48:51], v2, s[40:43], s5 offen offset:0 sc1\n"
"buffer_load_dwordx4 v[52:55], v2, s[40:43], s5 offen offset:1024 sc1\n"
"v_exp_f32 v234, v234\n"
"v_exp_f32 v235, v235\n"
"v_exp_f32 v236, v236\n"
"v_exp_f32 v237, v237\n"
"v_exp_f32 v238, v238\n"
"v_exp_f32 v239, v239\n"
"v_exp_f32 v208, v208\n"
"v_exp_f32 v209, v209\n"
"v_smfmac_f32_32x32x32_bf16 v[192:207], a[216:219], v[232:239], v45\n"
"v_exp_f32 v210, v210\n"
"v_exp_f32 v211, v211\n"
"v_exp_f32 v212, v212\n"
"v_mfma_f32_32x32x16_bf16 v[224:239], a[220:223], v[132:135], 0\n"
"v_exp_f32 v213, v213\n"
"v_exp_f32 v214, v214\n"
"v_exp_f32 v215, v215\n"
"v_exp_f32 v216, v216\n"
"v_exp_f32 v217, v217\n"
"v_smfmac_f32_32x32x32_bf16 v[144:159], a[224:227], v[208:215], v45\n"
"buffer_load_dwordx4 a[184:187], v2, s[40:43], s0 offen offset:0 sc1\n"
"buffer_load_dwordx4 a[196:199], v2, s[40:43], s0 offen offset:2048 sc1\n"
"buffer_load_dwordx4 v[56:59], v2, s[40:43], s5 offen offset:2048 sc1\n"
"buffer_load_dwordx4 v[60:63], v2, s[40:43], s5 offen offset:3072 sc1\n"
"v_exp_f32 v218, v218\n"
"v_exp_f32 v219, v219\n"
"v_exp_f32 v220, v220\n"
"v_exp_f32 v221, v221\n"
"v_exp_f32 v222, v222\n"
"v_exp_f32 v223, v223\n"
"v_exp_f32 v224, v224\n"
"v_exp_f32 v225, v225\n"
"v_smfmac_f32_32x32x32_bf16 v[144:159], a[228:231], v[216:223], v45\n"
"s_add_u32 s5, s5, 4096\n"
"v_exp_f32 v226, v226\n"
"v_exp_f32 v227, v227\n"
"v_exp_f32 v228, v228\n"
"v_mfma_f32_32x32x16_bf16 v[208:223], a[220:223], v[136:139], 0\n"
"v_exp_f32 v229, v229\n"
"v_exp_f32 v230, v230\n"
"v_exp_f32 v231, v231\n"
"v_exp_f32 v232, v232\n"
"v_exp_f32 v233, v233\n"
"v_smfmac_f32_32x32x32_bf16 v[160:175], a[224:227], v[224:231], v45\n"
"buffer_load_dwordx4 v[64:67], v2, s[40:43], s5 offen offset:0 sc1\n"
"buffer_load_dwordx4 v[68:71], v2, s[40:43], s5 offen offset:1024 sc1\n"
"v_exp_f32 v234, v234\n"
"v_exp_f32 v235, v235\n"
"v_exp_f32 v236, v236\n"
"v_exp_f32 v237, v237\n"
"v_exp_f32 v238, v238\n"
"v_exp_f32 v239, v239\n"
"v_exp_f32 v208, v208\n"
"v_exp_f32 v209, v209\n"
"v_smfmac_f32_32x32x32_bf16 v[160:175], a[228:231], v[232:239], v45\n"
"v_exp_f32 v210, v210\n"
"v_exp_f32 v211, v211\n"
"v_exp_f32 v212, v212\n"
"v_mfma_f32_32x32x16_bf16 v[224:239], a[220:223], v[140:143], 0\n"
"v_exp_f32 v213, v213\n"
"v_exp_f32 v214, v214\n"
"v_exp_f32 v215, v215\n"
"v_exp_f32 v216, v216\n"
"v_exp_f32 v217, v217\n"
"v_smfmac_f32_32x32x32_bf16 v[176:191], a[224:227], v[208:215], v45\n"
"buffer_load_dwordx4 v[72:75], v2, s[40:43], s5 offen offset:2048 sc1\n"
"buffer_load_dwordx4 v[76:79], v2, s[40:43], s5 offen offset:3072 sc1\n"
"v_exp_f32 v218, v218\n"
"v_exp_f32 v219, v219\n"
"v_exp_f32 v220, v220\n"
"v_exp_f32 v221, v221\n"
"v_exp_f32 v222, v222\n"
"v_exp_f32 v223, v223\n"
"v_exp_f32 v224, v224\n"
"v_exp_f32 v225, v225\n"
"v_smfmac_f32_32x32x32_bf16 v[176:191], a[228:231], v[216:223], v45\n"
"s_waitcnt vmcnt(49)\n"
"s_add_u32 s5, s5, 4096\n"
"v_exp_f32 v226, v226\n"
"v_exp_f32 v227, v227\n"
"v_exp_f32 v228, v228\n"
"v_mfma_f32_32x32x16_bf16 v[208:223], a[232:235], v[128:131], 0\n"
"v_exp_f32 v229, v229\n"
"v_exp_f32 v230, v230\n"
"v_exp_f32 v231, v231\n"
"v_exp_f32 v232, v232\n"
"v_exp_f32 v233, v233\n"
"v_smfmac_f32_32x32x32_bf16 v[192:207], a[224:227], v[224:231], v45\n"
"buffer_load_dwordx4 v[80:83], v2, s[40:43], s5 offen offset:0 sc1\n"
"buffer_load_dwordx4 v[84:87], v2, s[40:43], s5 offen offset:1024 sc1\n"
"v_exp_f32 v234, v234\n"
"v_exp_f32 v235, v235\n"
"v_exp_f32 v236, v236\n"
"v_exp_f32 v237, v237\n"
"v_exp_f32 v238, v238\n"
"v_exp_f32 v239, v239\n"
"v_exp_f32 v208, v208\n"
"v_exp_f32 v209, v209\n"
"v_smfmac_f32_32x32x32_bf16 v[192:207], a[228:231], v[232:239], v45\n"
"v_exp_f32 v210, v210\n"
"v_exp_f32 v211, v211\n"
"v_exp_f32 v212, v212\n"
"v_mfma_f32_32x32x16_bf16 v[224:239], a[232:235], v[132:135], 0\n"
"s_xor_b32 s0, s0, 16384\n"
"v_exp_f32 v213, v213\n"
"v_exp_f32 v214, v214\n"
"v_exp_f32 v215, v215\n"
"v_exp_f32 v216, v216\n"
"v_exp_f32 v217, v217\n"
"v_smfmac_f32_32x32x32_bf16 v[144:159], a[236:239], v[208:215], v45\n"
"s_mov_b64 exec, s[76:77]\n"
"buffer_load_dwordx4 a[212:215], v47, s[40:43], s0 offen offset:1024 sc1\n"
"buffer_load_dwordx4 a[216:219], v47, s[40:43], s0 offen offset:1536 sc1\n"
"buffer_load_dwordx4 a[224:227], v47, s[40:43], s0 offen offset:3072 sc1\n"
"buffer_load_dwordx4 a[228:231], v47, s[40:43], s0 offen offset:3584 sc1\n"
"s_mov_b64 exec, -1\n"
"v_exp_f32 v218, v218\n"
"v_exp_f32 v219, v219\n"
"v_exp_f32 v220, v220\n"
"v_exp_f32 v221, v221\n"
"v_exp_f32 v222, v222\n"
"v_exp_f32 v223, v223\n"
"v_exp_f32 v224, v224\n"
"v_exp_f32 v225, v225\n"
"v_smfmac_f32_32x32x32_bf16 v[144:159], a[240:243], v[216:223], v45\n"
"v_exp_f32 v226, v226\n"
"v_exp_f32 v227, v227\n"
"v_exp_f32 v228, v228\n"
"v_mfma_f32_32x32x16_bf16 v[208:223], a[232:235], v[136:139], 0\n"
"v_exp_f32 v229, v229\n"
"v_exp_f32 v230, v230\n"
"v_exp_f32 v231, v231\n"
"v_exp_f32 v232, v232\n"
"v_exp_f32 v233, v233\n"
"v_smfmac_f32_32x32x32_bf16 v[160:175], a[236:239], v[224:231], v45\n"
"buffer_load_dwordx4 a[208:211], v2, s[40:43], s0 offen offset:0 sc1\n"
"buffer_load_dwordx4 a[220:223], v2, s[40:43], s0 offen offset:2048 sc1\n"
"buffer_load_dwordx4 v[88:91], v2, s[40:43], s5 offen offset:2048 sc1\n"
"buffer_load_dwordx4 v[92:95], v2, s[40:43], s5 offen offset:3072 sc1\n"
"v_exp_f32 v234, v234\n"
"v_exp_f32 v235, v235\n"
"v_exp_f32 v236, v236\n"
"v_exp_f32 v237, v237\n"
"v_exp_f32 v238, v238\n"
"v_exp_f32 v239, v239\n"
"v_exp_f32 v208, v208\n"
"v_exp_f32 v209, v209\n"
"v_smfmac_f32_32x32x32_bf16 v[160:175], a[240:243], v[232:239], v45\n"
"s_waitcnt vmcnt(44)\n"
"s_add_u32 s5, s5, 4096\n"
"v_exp_f32 v210, v210\n"
"v_exp_f32 v211, v211\n"
"v_exp_f32 v212, v212\n"
"v_mfma_f32_32x32x16_bf16 v[224:239], a[232:235], v[140:143], 0\n"
"v_exp_f32 v213, v213\n"
"v_exp_f32 v214, v214\n"
"v_exp_f32 v215, v215\n"
"v_exp_f32 v216, v216\n"
"v_exp_f32 v217, v217\n"
"v_smfmac_f32_32x32x32_bf16 v[176:191], a[236:239], v[208:215], v45\n"
"buffer_load_dwordx4 v[96:99], v2, s[40:43], s5 offen offset:0 sc1\n"
"buffer_load_dwordx4 v[100:103], v2, s[40:43], s5 offen offset:1024 sc1\n"
"v_exp_f32 v218, v218\n"
"v_exp_f32 v219, v219\n"
"v_exp_f32 v220, v220\n"
"v_exp_f32 v221, v221\n"
"v_exp_f32 v222, v222\n"
"v_exp_f32 v223, v223\n"
"v_exp_f32 v224, v224\n"
"v_exp_f32 v225, v225\n"
"v_smfmac_f32_32x32x32_bf16 v[176:191], a[240:243], v[216:223], v45\n"
"v_exp_f32 v226, v226\n"
"v_exp_f32 v227, v227\n"
"v_exp_f32 v228, v228\n"
"v_mfma_f32_32x32x16_bf16 v[208:223], a[148:151], v[136:139], 0\n"
"v_exp_f32 v229, v229\n"
"v_exp_f32 v230, v230\n"
"v_exp_f32 v231, v231\n"
"v_exp_f32 v232, v232\n"
"v_exp_f32 v233, v233\n"
"v_smfmac_f32_32x32x32_bf16 v[192:207], a[236:239], v[224:231], v45\n"
"buffer_load_dwordx4 v[104:107], v2, s[40:43], s5 offen offset:2048 sc1\n"
"buffer_load_dwordx4 v[108:111], v2, s[40:43], s5 offen offset:3072 sc1\n"
"v_exp_f32 v234, v234\n"
"v_exp_f32 v235, v235\n"
"v_exp_f32 v236, v236\n"
"v_exp_f32 v237, v237\n"
"v_exp_f32 v238, v238\n"
"v_exp_f32 v239, v239\n"
"v_exp_f32 v208, v208\n"
"v_exp_f32 v209, v209\n"
"v_smfmac_f32_32x32x32_bf16 v[192:207], a[240:243], v[232:239], v45\n"
"v_exp_f32 v210, v210\n"
"v_exp_f32 v211, v211\n"
"v_exp_f32 v212, v212\n"
"v_mfma_f32_32x32x16_bf16 v[224:239], a[148:151], v[128:131], 0\n"
"s_xor_b32 s0, s0, 8192\n"
"v_exp_f32 v213, v213\n"
"v_exp_f32 v214, v214\n"
"v_exp_f32 v215, v215\n"
"v_exp_f32 v216, v216\n"
"v_exp_f32 v217, v217\n"
"v_smfmac_f32_32x32x32_bf16 v[176:191], a[152:155], v[208:215], v45\n"
"s_mov_b64 exec, s[76:77]\n"
"buffer_load_dwordx4 a[236:239], v47, s[40:43], s0 offen offset:1024 sc1\n"
"buffer_load_dwordx4 a[240:243], v47, s[40:43], s0 offen offset:1536 sc1\n"
"buffer_load_dwordx4 a[248:251], v47, s[40:43], s0 offen offset:3072 sc1\n"
"buffer_load_dwordx4 a[252:255], v47, s[40:43], s0 offen offset:3584 sc1\n"
"s_mov_b64 exec, -1\n"
"v_exp_f32 v218, v218\n"
"v_exp_f32 v219, v219\n"
"v_exp_f32 v220, v220\n"
"v_exp_f32 v221, v221\n"
"v_exp_f32 v222, v222\n"
"v_exp_f32 v223, v223\n"
"v_exp_f32 v224, v224\n"
"v_exp_f32 v225, v225\n"
"v_smfmac_f32_32x32x32_bf16 v[176:191], a[156:159], v[216:223], v45\n"
"s_waitcnt vmcnt(45)\n"
"s_add_u32 s6, s6, 4096\n"
"v_exp_f32 v226, v226\n"
"v_exp_f32 v227, v227\n"
"v_exp_f32 v228, v228\n"
"v_mfma_f32_32x32x16_bf16 v[208:223], a[160:163], v[136:139], 0\n"
"v_exp_f32 v229, v229\n"
"v_exp_f32 v230, v230\n"
"v_exp_f32 v231, v231\n"
"v_exp_f32 v232, v232\n"
"v_exp_f32 v233, v233\n"
"v_smfmac_f32_32x32x32_bf16 v[144:159], a[152:155], v[224:231], v45\n"
"buffer_load_dwordx4 a[232:235], v2, s[40:43], s0 offen offset:0 sc1\n"
"buffer_load_dwordx4 a[244:247], v2, s[40:43], s0 offen offset:2048 sc1\n"
"buffer_load_dwordx4 a[0:3], v2, s[40:43], s6 offen offset:0 sc1\n"
"buffer_load_dwordx4 a[4:7], v2, s[40:43], s6 offen offset:1024 sc1\n"
"v_exp_f32 v234, v234\n"
"v_exp_f32 v235, v235\n"
"v_exp_f32 v236, v236\n"
"v_exp_f32 v237, v237\n"
"v_exp_f32 v238, v238\n"
"v_exp_f32 v239, v239\n"
"v_exp_f32 v208, v208\n"
"v_exp_f32 v209, v209\n"
"v_smfmac_f32_32x32x32_bf16 v[144:159], a[156:159], v[232:239], v45\n"
"v_exp_f32 v210, v210\n"
"v_exp_f32 v211, v211\n"
"v_exp_f32 v212, v212\n"
"v_mfma_f32_32x32x16_bf16 v[224:239], a[160:163], v[128:131], 0\n"
"v_exp_f32 v213, v213\n"
"v_exp_f32 v214, v214\n"
"v_exp_f32 v215, v215\n"
"v_exp_f32 v216, v216\n"
"v_exp_f32 v217, v217\n"
"v_smfmac_f32_32x32x32_bf16 v[176:191], a[164:167], v[208:215], v45\n"
"buffer_load_dwordx4 a[8:11], v2, s[40:43], s6 offen offset:2048 sc1\n"
"buffer_load_dwordx4 a[12:15], v2, s[40:43], s6 offen offset:3072 sc1\n"
"v_exp_f32 v218, v218\n"
"v_exp_f32 v219, v219\n"
"v_exp_f32 v220, v220\n"
"v_exp_f32 v221, v221\n"
"v_exp_f32 v222, v222\n"
"v_exp_f32 v223, v223\n"
"v_exp_f32 v224, v224\n"
"v_exp_f32 v225, v225\n"
"v_smfmac_f32_32x32x32_bf16 v[176:191], a[168:171], v[216:223], v45\n"
"s_waitcnt vmcnt(50)\n"
"s_add_u32 s6, s6, 4096\n"
"v_exp_f32 v226, v226\n"
"v_exp_f32 v227, v227\n"
"v_exp_f32 v228, v228\n"
"v_mfma_f32_32x32x16_bf16 v[208:223], a[172:175], v[136:139], 0\n"
"v_exp_f32 v229, v229\n"
"v_exp_f32 v230, v230\n"
"v_exp_f32 v231, v231\n"
"v_exp_f32 v232, v232\n"
"v_exp_f32 v233, v233\n"
"v_smfmac_f32_32x32x32_bf16 v[144:159], a[164:167], v[224:231], v45\n"
"buffer_load_dwordx4 a[16:19], v2, s[40:43], s6 offen offset:0 sc1\n"
"buffer_load_dwordx4 a[20:23], v2, s[40:43], s6 offen offset:1024 sc1\n"
"v_exp_f32 v234, v234\n"
"v_exp_f32 v235, v235\n"
"v_exp_f32 v236, v236\n"
"v_exp_f32 v237, v237\n"
"v_exp_f32 v238, v238\n"
"v_exp_f32 v239, v239\n"
"v_exp_f32 v208, v208\n"
"v_exp_f32 v209, v209\n"
"v_smfmac_f32_32x32x32_bf16 v[144:159], a[168:171], v[232:239], v45\n"
"v_exp_f32 v210, v210\n"
"v_exp_f32 v211, v211\n"
"v_exp_f32 v212, v212\n"
"v_mfma_f32_32x32x16_bf16 v[224:239], a[172:175], v[128:131], 0\n"
"v_exp_f32 v213, v213\n"
"v_exp_f32 v214, v214\n"
"v_exp_f32 v215, v215\n"
"v_exp_f32 v216, v216\n"
"v_exp_f32 v217, v217\n"
"v_smfmac_f32_32x32x32_bf16 v[176:191], a[176:179], v[208:215], v45\n"
"buffer_load_dwordx4 a[24:27], v2, s[40:43], s6 offen offset:2048 sc1\n"
"buffer_load_dwordx4 a[28:31], v2, s[40:43], s6 offen offset:3072 sc1\n"
"v_exp_f32 v218, v218\n"
"v_exp_f32 v219, v219\n"
"v_exp_f32 v220, v220\n"
"v_exp_f32 v221, v221\n"
"v_exp_f32 v222, v222\n"
"v_exp_f32 v223, v223\n"
"v_exp_f32 v224, v224\n"
"v_exp_f32 v225, v225\n"
"v_smfmac_f32_32x32x32_bf16 v[176:191], a[180:183], v[216:223], v45\n"
"s_waitcnt vmcnt(35)\n"
"s_add_u32 s6, s6, 4096\n"
"v_exp_f32 v226, v226\n"
"v_exp_f32 v227, v227\n"
"v_exp_f32 v228, v228\n"
"v_mfma_f32_32x32x16_bf16 v[208:223], a[184:187], v[136:139], 0\n"
"v_exp_f32 v229, v229\n"
"v_exp_f32 v230, v230\n"
"v_exp_f32 v231, v231\n"
"v_exp_f32 v232, v232\n"
"v_exp_f32 v233, v233\n"
"v_smfmac_f32_32x32x32_bf16 v[144:159], a[176:179], v[224:231], v45\n"
"buffer_load_dwordx4 a[32:35], v2, s[40:43], s6 offen offset:0 sc1\n"
"buffer_load_dwordx4 a[36:39], v2, s[40:43], s6 offen offset:1024 sc1\n"
"v_exp_f32 v234, v234\n"
"v_exp_f32 v235, v235\n"
"v_exp_f32 v236, v236\n"
"v_exp_f32 v237, v237\n"
"v_exp_f32 v238, v238\n"
"v_exp_f32 v239, v239\n"
"v_exp_f32 v208, v208\n"
"v_exp_f32 v209, v209\n"
"v_smfmac_f32_32x32x32_bf16 v[144:159], a[180:183], v[232:239], v45\n"
"v_exp_f32 v210, v210\n"
"v_exp_f32 v211, v211\n"
"v_exp_f32 v212, v212\n"
"v_mfma_f32_32x32x16_bf16 v[224:239], a[184:187], v[128:131], 0\n"
"v_exp_f32 v213, v213\n"
"v_exp_f32 v214, v214\n"
"v_exp_f32 v215, v215\n"
"v_exp_f32 v216, v216\n"
"v_exp_f32 v217, v217\n"
"v_smfmac_f32_32x32x32_bf16 v[176:191], a[188:191], v[208:215], v45\n"
"buffer_load_dwordx4 a[40:43], v2, s[40:43], s6 offen offset:2048 sc1\n"
"buffer_load_dwordx4 a[44:47], v2, s[40:43], s6 offen offset:3072 sc1\n"
"v_exp_f32 v218, v218\n"
"v_exp_f32 v219, v219\n"
"v_exp_f32 v220, v220\n"
"v_exp_f32 v221, v221\n"
"v_exp_f32 v222, v222\n"
"v_exp_f32 v223, v223\n"
"v_exp_f32 v224, v224\n"
"v_exp_f32 v225, v225\n"
"v_smfmac_f32_32x32x32_bf16 v[176:191], a[192:195], v[216:223], v45\n"
"s_waitcnt vmcnt(38)\n"
"s_add_u32 s6, s6, 4096\n"
"v_exp_f32 v226, v226\n"
"v_exp_f32 v227, v227\n"
"v_exp_f32 v228, v228\n"
"v_mfma_f32_32x32x16_bf16 v[208:223], a[196:199], v[136:139], 0\n"
"v_exp_f32 v229, v229\n"
"v_exp_f32 v230, v230\n"
"v_exp_f32 v231, v231\n"
"v_exp_f32 v232, v232\n"
"v_exp_f32 v233, v233\n"
"v_smfmac_f32_32x32x32_bf16 v[144:159], a[188:191], v[224:231], v45\n"
"buffer_load_dwordx4 a[48:51], v2, s[40:43], s6 offen offset:0 sc1\n"
"buffer_load_dwordx4 a[52:55], v2, s[40:43], s6 offen offset:1024 sc1\n"
"v_exp_f32 v234, v234\n"
"v_exp_f32 v235, v235\n"
"v_exp_f32 v236, v236\n"
"v_exp_f32 v237, v237\n"
"v_exp_f32 v238, v238\n"
"v_exp_f32 v239, v239\n"
"v_exp_f32 v208, v208\n"
"v_exp_f32 v209, v209\n"
"v_smfmac_f32_32x32x32_bf16 v[144:159], a[192:195], v[232:239], v45\n"
"v_exp_f32 v210, v210\n"
"v_exp_f32 v211, v211\n"
"v_exp_f32 v212, v212\n"
"v_mfma_f32_32x32x16_bf16 v[224:239], a[196:199], v[128:131], 0\n"
"v_exp_f32 v213, v213\n"
"v_exp_f32 v214, v214\n"
"v_exp_f32 v215, v215\n"
"v_exp_f32 v216, v216\n"
"v_exp_f32 v217, v217\n"
"v_smfmac_f32_32x32x32_bf16 v[176:191], a[200:203], v[208:215], v45\n"
"buffer_load_dwordx2 v[32:33], v255, s[40:43], 0 offen offset:136 sc1\n"
"buffer_load_dwordx4 a[56:59], v2, s[40:43], s6 offen offset:2048 sc1\n"
"buffer_load_dwordx4 a[60:63], v2, s[40:43], s6 offen offset:3072 sc1\n"
"v_exp_f32 v218, v218\n"
"v_exp_f32 v219, v219\n"
"v_exp_f32 v220, v220\n"
"v_exp_f32 v221, v221\n"
"v_exp_f32 v222, v222\n"
"v_exp_f32 v223, v223\n"
"v_exp_f32 v224, v224\n"
"v_exp_f32 v225, v225\n"
"v_smfmac_f32_32x32x32_bf16 v[176:191], a[204:207], v[216:223], v45\n"
"s_waitcnt vmcnt(30)\n"
"s_add_u32 s6, s6, 4096\n"
"v_exp_f32 v226, v226\n"
"v_exp_f32 v227, v227\n"
"v_exp_f32 v228, v228\n"
"v_mfma_f32_32x32x16_bf16 v[208:223], a[208:211], v[136:139], 0\n"
"v_exp_f32 v229, v229\n"
"v_exp_f32 v230, v230\n"
"v_exp_f32 v231, v231\n"
"v_exp_f32 v232, v232\n"
"v_exp_f32 v233, v233\n"
"v_smfmac_f32_32x32x32_bf16 v[144:159], a[200:203], v[224:231], v45\n"
"buffer_load_dwordx4 a[64:67], v2, s[40:43], s6 offen offset:0 sc1\n"
"buffer_load_dwordx4 a[68:71], v2, s[40:43], s6 offen offset:1024 sc1\n"
"v_exp_f32 v234, v234\n"
"v_exp_f32 v235, v235\n"
"v_exp_f32 v236, v236\n"
"v_exp_f32 v237, v237\n"
"v_exp_f32 v238, v238\n"
"v_exp_f32 v239, v239\n"
"v_exp_f32 v208, v208\n"
"v_exp_f32 v209, v209\n"
"v_smfmac_f32_32x32x32_bf16 v[144:159], a[204:207], v[232:239], v45\n"
"v_exp_f32 v210, v210\n"
"v_exp_f32 v211, v211\n"
"v_exp_f32 v212, v212\n"
"v_mfma_f32_32x32x16_bf16 v[224:239], a[208:211], v[128:131], 0\n"
"v_exp_f32 v213, v213\n"
"v_exp_f32 v214, v214\n"
"v_exp_f32 v215, v215\n"
"v_exp_f32 v216, v216\n"
"v_exp_f32 v217, v217\n"
"v_smfmac_f32_32x32x32_bf16 v[176:191], a[212:215], v[208:215], v45\n"
"buffer_load_dwordx4 a[72:75], v2, s[40:43], s6 offen offset:2048 sc1\n"
"buffer_load_dwordx4 a[76:79], v2, s[40:43], s6 offen offset:3072 sc1\n"
"v_exp_f32 v218, v218\n"
"v_exp_f32 v219, v219\n"
"v_exp_f32 v220, v220\n"
"v_exp_f32 v221, v221\n"
"v_exp_f32 v222, v222\n"
"v_exp_f32 v223, v223\n"
"v_exp_f32 v224, v224\n"
"v_exp_f32 v225, v225\n"
"v_smfmac_f32_32x32x32_bf16 v[176:191], a[216:219], v[216:223], v45\n"
"s_waitcnt vmcnt(33)\n"
"s_add_u32 s6, s6, 4096\n"
"v_exp_f32 v226, v226\n"
"v_exp_f32 v227, v227\n"
"v_exp_f32 v228, v228\n"
"v_mfma_f32_32x32x16_bf16 v[208:223], a[220:223], v[136:139], 0\n"
"v_exp_f32 v229, v229\n"
"v_exp_f32 v230, v230\n"
"v_exp_f32 v231, v231\n"
"v_exp_f32 v232, v232\n"
"v_exp_f32 v233, v233\n"
"v_smfmac_f32_32x32x32_bf16 v[144:159], a[212:215], v[224:231], v45\n"
"buffer_load_dwordx4 a[80:83], v2, s[40:43], s6 offen offset:0 sc1\n"
"buffer_load_dwordx4 a[84:87], v2, s[40:43], s6 offen offset:1024 sc1\n"
"v_exp_f32 v234, v234\n"
"v_exp_f32 v235, v235\n"
"v_exp_f32 v236, v236\n"
"v_exp_f32 v237, v237\n"
"v_exp_f32 v238, v238\n"
"v_exp_f32 v239, v239\n"
"v_exp_f32 v208, v208\n"
"v_exp_f32 v209, v209\n"
"v_smfmac_f32_32x32x32_bf16 v[144:159], a[216:219], v[232:239], v45\n"
"v_exp_f32 v210, v210\n"
"v_exp_f32 v211, v211\n"
"v_exp_f32 v212, v212\n"
"v_mfma_f32_32x32x16_bf16 v[224:239], a[220:223], v[128:131], 0\n"
"v_exp_f32 v213, v213\n"
"v_exp_f32 v214, v214\n"
"v_exp_f32 v215, v215\n"
"v_exp_f32 v216, v216\n"
"v_exp_f32 v217, v217\n"
"v_smfmac_f32_32x32x32_bf16 v[176:191], a[224:227], v[208:215], v45\n"
"buffer_load_dwordx4 a[88:91], v2, s[40:43], s6 offen offset:2048 sc1\n"
"buffer_load_dwordx4 a[92:95], v2, s[40:43], s6 offen offset:3072 sc1\n"
"v_exp_f32 v218, v218\n"
"v_exp_f32 v219, v219\n"
"v_exp_f32 v220, v220\n"
"v_exp_f32 v221, v221\n"
"v_exp_f32 v222, v222\n"
"v_exp_f32 v223, v223\n"
"v_exp_f32 v224, v224\n"
"v_exp_f32 v225, v225\n"
"v_smfmac_f32_32x32x32_bf16 v[176:191], a[228:231], v[216:223], v45\n"
"s_waitcnt vmcnt(26)\n"
"s_add_u32 s6, s6, 4096\n"
"v_exp_f32 v226, v226\n"
"v_exp_f32 v227, v227\n"
"v_exp_f32 v228, v228\n"
"v_mfma_f32_32x32x16_bf16 v[208:223], a[232:235], v[136:139], 0\n"
"v_exp_f32 v229, v229\n"
"v_exp_f32 v230, v230\n"
"v_exp_f32 v231, v231\n"
"v_exp_f32 v232, v232\n"
"v_exp_f32 v233, v233\n"
"v_smfmac_f32_32x32x32_bf16 v[144:159], a[224:227], v[224:231], v45\n"
"buffer_load_dwordx4 a[96:99], v2, s[40:43], s6 offen offset:0 sc1\n"
"buffer_load_dwordx4 a[100:103], v2, s[40:43], s6 offen offset:1024 sc1\n"
"v_exp_f32 v234, v234\n"
"v_exp_f32 v235, v235\n"
"v_exp_f32 v236, v236\n"
"v_exp_f32 v237, v237\n"
"v_exp_f32 v238, v238\n"
"v_exp_f32 v239, v239\n"
"v_exp_f32 v208, v208\n"
"v_exp_f32 v209, v209\n"
"v_smfmac_f32_32x32x32_bf16 v[144:159], a[228:231], v[232:239], v45\n"
"s_waitcnt vmcnt(27)\n"
"v_exp_f32 v210, v210\n"
"v_exp_f32 v211, v211\n"
"v_exp_f32 v212, v212\n"
"v_mfma_f32_32x32x16_bf16 v[224:239], a[244:247], v[136:139], 0\n"
"v_exp_f32 v213, v213\n"
"v_exp_f32 v214, v214\n"
"v_exp_f32 v215, v215\n"
"v_exp_f32 v216, v216\n"
"v_exp_f32 v217, v217\n"
"v_smfmac_f32_32x32x32_bf16 v[176:191], a[236:239], v[208:215], v45\n"
"L_pad7:\n"
"s_cbranch_execz L_ret10\n"
"buffer_load_dwordx4 a[104:107], v2, s[40:43], s6 offen offset:2048 sc1\n"
"buffer_load_dwordx4 a[108:111], v2, s[40:43], s6 offen offset:3072 sc1\n"
"v_exp_f32 v218, v218\n"
"v_exp_f32 v219, v219\n"
"v_exp_f32 v220, v220\n"
"v_exp_f32 v221, v221\n"
"v_exp_f32 v222, v222\n"
"v_exp_f32 v223, v223\n"
"v_exp_f32 v224, v224\n"
"v_exp_f32 v225, v225\n"
"v_smfmac_f32_32x32x32_bf16 v[176:191], a[240:243], v[216:223], v45\n"
"s_add_u32 s6, s6, 4096\n"
"v_exp_f32 v226, v226\n"
"v_exp_f32 v227, v227\n"
"v_exp_f32 v228, v228\n"
"v_mfma_f32_32x32x16_bf16 v[208:223], a[232:235], v[128:131], 0\n"
"v_exp_f32 v229, v229\n"
"v_exp_f32 v230, v230\n"
"v_exp_f32 v231, v231\n"
"v_exp_f32 v232, v232\n"
"v_exp_f32 v233, v233\n"
"v_smfmac_f32_32x32x32_bf16 v[176:191], a[248:251], v[224:231], v45\n"
"buffer_load_dwordx4 a[112:115], v2, s[40:43], s6 offen offset:0 sc1\n"
"buffer_load_dwordx4 a[116:119], v2, s[40:43], s6 offen offset:1024 sc1\n"
"v_exp_f32 v234, v234\n"
"v_exp_f32 v235, v235\n"
"v_exp_f32 v236, v236\n"
"v_exp_f32 v237, v237\n"
"v_exp_f32 v238, v238\n"
"v_exp_f32 v239, v239\n"
"v_exp_f32 v208, v208\n"
"v_exp_f32 v209, v209\n"
"v_smfmac_f32_32x32x32_bf16 v[176:191], a[252:255], v[232:239], v45\n"
"v_exp_f32 v210, v210\n"
"v_exp_f32 v211, v211\n"
"v_exp_f32 v212, v212\n"
"v_mfma_f32_32x32x16_bf16 v[224:239], a[244:247], v[128:131], 0\n"
"v_exp_f32 v213, v213\n"
"v_exp_f32 v214, v214\n"
"v_exp_f32 v215, v215\n"
"v_exp_f32 v216, v216\n"
"v_exp_f32 v217, v217\n"
"v_smfmac_f32_32x32x32_bf16 v[144:159], a[236:239], v[208:215], v45\n"
"buffer_load_dwordx4 a[120:123], v2, s[40:43], s6 offen offset:2048 sc1\n"
"buffer_load_dwordx4 a[124:127], v2, s[40:43], s6 offen offset:3072 sc1\n"
"v_exp_f32 v218, v218\n"
"v_exp_f32 v219, v219\n"
"v_exp_f32 v220, v220\n"
"v_exp_f32 v221, v221\n"
"v_exp_f32 v222, v222\n"
"ds_write_b128 v41, v[176:179] offset:0\n"
"ds_write_b128 v41, v[180:183] offset:1024\n"
"ds_write_b32 v42, v184 offset:2048\n"
"v_exp_f32 v223, v223\n"
"v_exp_f32 v224, v224\n"
"v_exp_f32 v225, v225\n"
"v_smfmac_f32_32x32x32_bf16 v[144:159], a[240:243], v[216:223], v45\n"
"s_add_u32 s6, s6, 4096\n"
"v_exp_f32 v226, v226\n"
"v_exp_f32 v227, v227\n"
"v_exp_f32 v228, v228\n"
"v_mfma_f32_32x32x16_bf16 v[208:223], a[148:151], v[140:143], 0\n"
"v_exp_f32 v229, v229\n"
"v_exp_f32 v230, v230\n"
"v_exp_f32 v231, v231\n"
"v_exp_f32 v232, v232\n"
"v_exp_f32 v233, v233\n"
"v_smfmac_f32_32x32x32_bf16 v[144:159], a[248:251], v[224:231], v45\n"
"buffer_load_dwordx4 a[128:131], v2, s[40:43], s6 offen offset:0 sc1\n"
"buffer_load_dwordx4 a[132:135], v2, s[40:43], s6 offen offset:1024 sc1\n"
"v_exp_f32 v234, v234\n"
"v_exp_f32 v235, v235\n"
"v_exp_f32 v236, v236\n"
"v_exp_f32 v237, v237\n"
"v_exp_f32 v238, v238\n"
"s_waitcnt lgkmcnt(0)\n"
"s_barrier\n"
"ds_read_b32 v28, v126 offset:2048\n"
"ds_read_b128 v[20:23], v43 offset:0\n"
"ds_read_b128 v[24:27], v43 offset:1024\n"
"v_exp_f32 v239, v239\n"
"v_exp_f32 v208, v208\n"
"v_exp_f32 v209, v209\n"
"v_smfmac_f32_32x32x32_bf16 v[144:159], a[252:255], v[232:239], v45\n"
"v_exp_f32 v210, v210\n"
"v_exp_f32 v211, v211\n"
"v_exp_f32 v212, v212\n"
"v_mfma_f32_32x32x16_bf16 v[224:239], a[148:151], v[132:135], 0\n"
"v_exp_f32 v213, v213\n"
"v_exp_f32 v214, v214\n"
"v_exp_f32 v215, v215\n"
"v_exp_f32 v216, v216\n"
"v_exp_f32 v217, v217\n"
"v_smfmac_f32_32x32x32_bf16 v[192:207], a[152:155], v[208:215], v45\n"
"buffer_load_dwordx4 a[136:139], v2, s[40:43], s6 offen offset:2048 sc1\n"
"buffer_load_dwordx4 a[140:143], v2, s[40:43], s6 offen offset:3072 sc1\n"
"v_exp_f32 v218, v218\n"
"v_exp_f32 v219, v219\n"
"v_exp_f32 v220, v220\n"
"v_exp_f32 v221, v221\n"
"v_exp_f32 v222, v222\n"
"s_waitcnt lgkmcnt(0)\n"
"v_add_f32 v152, v152, v28\n"
"v_rcp_f32 v152, v152\n"
"v_pk_add_f32 v[144:145], v[144:145], v[20:21]\n"
"v_pk_add_f32 v[146:147], v[146:147], v[22:23]\n"
"v_pk_add_f32 v[148:149], v[148:149], v[24:25]\n"
"v_pk_add_f32 v[150:151], v[150:151], v[26:27]\n"
"v_pk_mul_f32 v[144:145], v[144:145], v[152:153] op_sel_hi:[1,0]\n"
"v_pk_mul_f32 v[146:147], v[146:147], v[152:153] op_sel_hi:[1,0]\n"
"v_pk_mul_f32 v[148:149], v[148:149], v[152:153] op_sel_hi:[1,0]\n"
"v_pk_mul_f32 v[150:151], v[150:151], v[152:153] op_sel_hi:[1,0]\n"
"v_cvt_pk_bf16_f32 v20, v144, v145\n"
"v_cvt_pk_bf16_f32 v21, v146, v147\n"
"v_cvt_pk_bf16_f32 v22, v148, v149\n"
"v_cvt_pk_bf16_f32 v23, v150, v151\n"
"buffer_store_dwordx4 v[20:23], v2, s[40:43], s32 offen sc1\n"
"v_exp_f32 v223, v223\n"
"v_exp_f32 v224, v224\n"
"v_exp_f32 v225, v225\n"
"v_smfmac_f32_32x32x32_bf16 v[192:207], a[156:159], v[216:223], v45\n"
"v_exp_f32 v226, v226\n"
"v_exp_f32 v227, v227\n"
"v_exp_f32 v228, v228\n"
"v_mfma_f32_32x32x16_bf16 v[208:223], a[160:163], v[140:143], 0\n"
"v_exp_f32 v229, v229\n"
"v_exp_f32 v230, v230\n"
"v_exp_f32 v231, v231\n"
"v_exp_f32 v232, v232\n"
"v_exp_f32 v233, v233\n"
"v_smfmac_f32_32x32x32_bf16 v[160:175], a[152:155], v[224:231], v45\n"
"v_exp_f32 v234, v234\n"
"v_exp_f32 v235, v235\n"
"v_exp_f32 v236, v236\n"
"v_exp_f32 v237, v237\n"
"v_exp_f32 v238, v238\n"
"v_pk_add_f32 v[8:9], v[144:145], v[146:147]\n"
"v_pk_add_f32 v[8:9], v[8:9], v[148:149]\n"
"v_pk_add_f32 v[8:9], v[8:9], v[150:151]\n"
"v_fmac_f32 v8, 0x70800000, v152\n"
"v_add_f32 v8, v8, v9\n"
"v_fmac_f32 v152, 0, v8\n"
"v_cmpx_nle_f32 vcc, 0x21000000, v152\n"
"ds_write_b32 v40, v45 offset:41984\n"
"s_mov_b64 exec, -1\n"
"v_exp_f32 v239, v239\n"
"v_exp_f32 v208, v208\n"
"v_exp_f32 v209, v209\n"
"v_smfmac_f32_32x32x32_bf16 v[160:175], a[156:159], v[232:239], v45\n"
"s_add_u32 s6, s6, 4096\n"
"v_exp_f32 v210, v210\n"
"v_exp_f32 v211, v211\n"
"v_exp_f32 v212, v212\n"
"v_mfma_f32_32x32x16_bf16 v[224:239], a[160:163], v[132:135], 0\n"
"v_exp_f32 v213, v213\n"
"v_exp_f32 v214, v214\n"
"v_exp_f32 v215, v215\n"
"v_exp_f32 v216, v216\n"
"v_exp_f32 v217, v217\n"
"v_smfmac_f32_32x32x32_bf16 v[192:207], a[164:167], v[208:215], v45\n"
"buffer_load_dwordx4 a[144:147], v2, s[40:43], s6 offen offset:0 sc1\n"
"buffer_load_dwordx4 a[148:151], v2, s[40:43], s6 offen offset:1024 sc1\n"
"v_exp_f32 v218, v218\n"
"v_exp_f32 v219, v219\n"
"v_exp_f32 v220, v220\n"
"v_exp_f32 v221, v221\n"
"v_exp_f32 v222, v222\n"
"v_exp_f32 v223, v223\n"
"v_exp_f32 v224, v224\n"
"v_exp_f32 v225, v225\n"
"v_smfmac_f32_32x32x32_bf16 v[192:207], a[168:171], v[216:223], v45\n"
"v_exp_f32 v226, v226\n"
"v_exp_f32 v227, v227\n"
"v_exp_f32 v228, v228\n"
"v_mfma_f32_32x32x16_bf16 v[208:223], a[172:175], v[140:143], 0\n"
"v_exp_f32 v229, v229\n"
"v_exp_f32 v230, v230\n"
"v_exp_f32 v231, v231\n"
"v_exp_f32 v232, v232\n"
"v_exp_f32 v233, v233\n"
"v_smfmac_f32_32x32x32_bf16 v[160:175], a[164:167], v[224:231], v45\n"
"buffer_load_dwordx4 a[152:155], v2, s[40:43], s6 offen offset:2048 sc1\n"
"buffer_load_dwordx4 a[156:159], v2, s[40:43], s6 offen offset:3072 sc1\n"
"v_exp_f32 v234, v234\n"
"v_exp_f32 v235, v235\n"
"v_exp_f32 v236, v236\n"
"v_exp_f32 v237, v237\n"
"v_exp_f32 v238, v238\n"
"v_exp_f32 v239, v239\n"
"v_exp_f32 v208, v208\n"
"v_exp_f32 v209, v209\n"
"v_smfmac_f32_32x32x32_bf16 v[160:175], a[168:171], v[232:239], v45\n"
"s_add_u32 s6, s6, 4096\n"
"v_exp_f32 v210, v210\n"
"v_exp_f32 v211, v211\n"
"v_exp_f32 v212, v212\n"
"v_mfma_f32_32x32x16_bf16 v[224:239], a[172:175], v[132:135], 0\n"
"v_exp_f32 v213, v213\n"
"v_exp_f32 v214, v214\n"
"v_exp_f32 v215, v215\n"
"v_exp_f32 v216, v216\n"
"v_exp_f32 v217, v217\n"
"v_smfmac_f32_32x32x32_bf16 v[192:207], a[176:179], v[208:215], v45\n"
"buffer_load_dwordx4 a[160:163], v2, s[40:43], s6 offen offset:0 sc1\n"
"buffer_load_dwordx4 a[164:167], v2, s[40:43], s6 offen offset:1024 sc1\n"
"v_exp_f32 v218, v218\n"
"v_exp_f32 v219, v219\n"
"v_exp_f32 v220, v220\n"
"v_exp_f32 v221, v221\n"
"v_exp_f32 v222, v222\n"
"v_exp_f32 v223, v223\n"
"v_exp_f32 v224, v224\n"
"v_exp_f32 v225, v225\n"
"v_smfmac_f32_32x32x32_bf16 v[192:207], a[180:183], v[216:223], v45\n"
"v_exp_f32 v226, v226\n"
"v_exp_f32 v227, v227\n"
"v_exp_f32 v228, v228\n"
"v_mfma_f32_32x32x16_bf16 v[208:223], a[184:187], v[140:143], 0\n"
"v_exp_f32 v229, v229\n"
"v_exp_f32 v230, v230\n"
"v_exp_f32 v231, v231\n"
"v_exp_f32 v232, v232\n"
"v_exp_f32 v233, v233\n"
"v_smfmac_f32_32x32x32_bf16 v[160:175], a[176:179], v[224:231], v45\n"
"v_exp_f32 v234, v234\n"
"v_exp_f32 v235, v235\n"
"v_exp_f32 v236, v236\n"
"v_exp_f32 v237, v237\n"
"v_exp_f32 v238, v238\n"
"v_exp_f32 v239, v239\n"
"v_exp_f32 v208, v208\n"
"v_exp_f32 v209, v209\n"
"v_smfmac_f32_32x32x32_bf16 v[160:175], a[180:183], v[232:239], v45\n"
"s_waitcnt vmcnt(29)\n"
"v_cmp_ne_u64 vcc, s[58:59], v[32:33]\n"
"s_cbranch_vccnz L_rslow\n"
"L_rback:\n"
"s_cmp_eq_u32 s7, 0\n"
"s_cbranch_scc1 L_ep\n"
"L_epb:\n"
"v_exp_f32 v210, v210\n"
"v_exp_f32 v211, v211\n"
"v_exp_f32 v212, v212\n"
"v_mfma_f32_32x32x16_bf16 v[224:239], a[184:187], v[132:135], 0\n"
"v_exp_f32 v213, v213\n"
"v_exp_f32 v214, v214\n"
"v_exp_f32 v215, v215\n"
"v_exp_f32 v216, v216\n"
"v_exp_f32 v217, v217\n"
"v_smfmac_f32_32x32x32_bf16 v[192:207], a[188:191], v[208:215], v45\n"
"buffer_load_dwordx4 a[168:171], v2, s[40:43], s6 offen offset:2048 sc1\n"
"buffer_load_dwordx4 a[172:175], v2, s[40:43], s6 offen offset:3072 sc1\n"
"v_exp_f32 v218, v218\n"
"v_exp_f32 v219, v219\n"
"v_exp_f32 v220, v220\n"
"v_exp_f32 v221, v221\n"
"v_exp_f32 v222, v222\n"
"v_exp_f32 v223, v223\n"
"v_exp_f32 v224, v224\n"
"v_exp_f32 v225, v225\n"
"v_smfmac_f32_32x32x32_bf16 v[192:207], a[192:195], v[216:223], v45\n"
"s_add_u32 s6, s6, 4096\n"
"v_exp_f32 v226, v226\n"
"v_exp_f32 v227, v227\n"
"v_exp_f32 v228, v228\n"
"v_mfma_f32_32x32x16_bf16 v[208:223], a[196:199], v[140:143], 0\n"
"v_exp_f32 v229, v229\n"
"v_exp_f32 v230, v230\n"
"v_exp_f32 v231, v231\n"
"v_exp_f32 v232, v232\n"
"v_exp_f32 v233, v233\n"
"v_smfmac_f32_32x32x32_bf16 v[160:175], a[188:191], v[224:231], v45\n"
"buffer_load_dwordx4 a[176:179], v2, s[40:43], s6 offen offset:0 sc1\n"
"buffer_load_dwordx4 a[180:183], v2, s[40:43], s6 offen offset:1024 sc1\n"
"v_exp_f32 v234, v234\n"
"v_exp_f32 v235, v235\n"
"v_exp_f32 v236, v236\n"
"v_exp_f32 v237, v237\n"
"v_exp_f32 v238, v238\n"
"v_exp_f32 v239, v239\n"
"v_exp_f32 v208, v208\n"
"v_exp_f32 v209, v209\n"
"v_smfmac_f32_32x32x32_bf16 v[160:175], a[192:195], v[232:239], v45\n"
"v_exp_f32 v210, v210\n"
"v_exp_f32 v211, v211\n"
"v_exp_f32 v212, v212\n"
"v_mfma_f32_32x32x16_bf16 v[224:239], a[196:199], v[132:135], 0\n"
"v_exp_f32 v213, v213\n"
"v_exp_f32 v214, v214\n"
"v_exp_f32 v215, v215\n"
"v_exp_f32 v216, v216\n"
"v_exp_f32 v217, v217\n"
"v_smfmac_f32_32x32x32_bf16 v[192:207], a[200:203], v[208:215], v45\n"
"s_waitcnt lgkmcnt(0)\n"
"s_waitcnt vmcnt(10)\n"
"s_barrier\n"
"ds_read_b32 v29, v40 offset:41984\n"
"buffer_load_dwordx4 a[184:187], v2, s[40:43], s6 offen offset:2048 sc1\n"
"buffer_load_dwordx4 a[188:191], v2, s[40:43], s6 offen offset:3072 sc1\n"
"v_exp_f32 v218, v218\n"
"v_exp_f32 v219, v219\n"
"v_exp_f32 v220, v220\n"
"v_exp_f32 v221, v221\n"
"v_exp_f32 v222, v222\n"
"s_waitcnt lgkmcnt(0)\n"
"v_cmp_eq_u32 vcc, 0, v29\n"
"v_readfirstlane_b32 s69, v29\n"
"s_nop 0\n"
"v_cndmask_b32 v29, 0, v7, vcc\n"
"buffer_atomic_add v29, v3, s[40:43], 0 offen offset:256\n"
"v_exp_f32 v223, v223\n"
"v_exp_f32 v224, v224\n"
"v_exp_f32 v225, v225\n"
"v_smfmac_f32_32x32x32_bf16 v[192:207], a[204:207], v[216:223], v45\n"
"v_exp_f32 v226, v226\n"
"v_exp_f32 v227, v227\n"
"v_exp_f32 v228, v228\n"
"v_mfma_f32_32x32x16_bf16 v[208:223], a[208:211], v[140:143], 0\n"
"v_exp_f32 v229, v229\n"
"v_exp_f32 v230, v230\n"
"v_exp_f32 v231, v231\n"
"v_exp_f32 v232, v232\n"
"v_exp_f32 v233, v233\n"
"v_smfmac_f32_32x32x32_bf16 v[160:175], a[200:203], v[224:231], v45\n"
"v_exp_f32 v234, v234\n"
"v_exp_f32 v235, v235\n"
"v_exp_f32 v236, v236\n"
"v_exp_f32 v237, v237\n"
"v_exp_f32 v238, v238\n"
"v_exp_f32 v239, v239\n"
"v_exp_f32 v208, v208\n"
"v_exp_f32 v209, v209\n"
"v_smfmac_f32_32x32x32_bf16 v[160:175], a[204:207], v[232:239], v45\n"
"s_add_u32 s6, s6, 4096\n"
"v_exp_f32 v210, v210\n"
"v_exp_f32 v211, v211\n"
"v_exp_f32 v212, v212\n"
"v_mfma_f32_32x32x16_bf16 v[224:239], a[208:211], v[132:135], 0\n"
"v_exp_f32 v213, v213\n"
"v_exp_f32 v214, v214\n"
"v_exp_f32 v215, v215\n"
"v_exp_f32 v216, v216\n"
"v_exp_f32 v217, v217\n"
"v_smfmac_f32_32x32x32_bf16 v[192:207], a[212:215], v[208:215], v45\n"
"buffer_load_dwordx4 a[192:195], v2, s[40:43], s6 offen offset:0 sc1\n"
"buffer_load_dwordx4 a[196:199], v2, s[40:43], s6 offen offset:1024 sc1\n"
"v_exp_f32 v218, v218\n"
"v_exp_f32 v219, v219\n"
"v_exp_f32 v220, v220\n"
"v_exp_f32 v221, v221\n"
"v_exp_f32 v222, v222\n"
"v_exp_f32 v223, v223\n"
"v_exp_f32 v224, v224\n"
"v_exp_f32 v225, v225\n"
"v_smfmac_f32_32x32x32_bf16 v[192:207], a[216:219], v[216:223], v45\n"
"v_exp_f32 v226, v226\n"
"v_exp_f32 v227, v227\n"
"v_exp_f32 v228, v228\n"
"v_mfma_f32_32x32x16_bf16 v[208:223], a[220:223], v[140:143], 0\n"
"v_exp_f32 v229, v229\n"
"v_exp_f32 v230, v230\n"
"v_exp_f32 v231, v231\n"
"v_exp_f32 v232, v232\n"
"v_exp_f32 v233, v233\n"
"v_smfmac_f32_32x32x32_bf16 v[160:175], a[212:215], v[224:231], v45\n"
"buffer_load_dwordx4 a[200:203], v2, s[40:43], s6 offen offset:2048 sc1\n"
"buffer_load_dwordx4 a[204:207], v2, s[40:43], s6 offen offset:3072 sc1\n"
"v_exp_f32 v234, v234\n"
"v_exp_f32 v235, v235\n"
"v_exp_f32 v236, v236\n"
"v_exp_f32 v237, v237\n"
"v_exp_f32 v238, v238\n"
"v_exp_f32 v239, v239\n"
"v_exp_f32 v208, v208\n"
"v_exp_f32 v209, v209\n"
"v_smfmac_f32_32x32x32_bf16 v[160:175], a[216:219], v[232:239], v45\n"
"s_add_u32 s6, s6, 4096\n"
"v_exp_f32 v210, v210\n"
"v_exp_f32 v211, v211\n"
"v_exp_f32 v212, v212\n"
"v_mfma_f32_32x32x16_bf16 v[224:239], a[220:223], v[132:135], 0\n"
"v_exp_f32 v213, v213\n"
"v_exp_f32 v214, v214\n"
"v_exp_f32 v215, v215\n"
"v_exp_f32 v216, v216\n"
"v_exp_f32 v217, v217\n"
"v_smfmac_f32_32x32x32_bf16 v[192:207], a[224:227], v[208:215], v45\n"
"buffer_load_dwordx4 a[208:211], v2, s[40:43], s6 offen offset:0 sc1\n"
"buffer_load_dwordx4 a[212:215], v2, s[40:43], s6 offen offset:1024 sc1\n"
"v_exp_f32 v218, v218\n"
"v_exp_f32 v219, v219\n"
"v_exp_f32 v220, v220\n"
"v_exp_f32 v221, v221\n"
"v_exp_f32 v222, v222\n"
"v_exp_f32 v223, v223\n"
"v_exp_f32 v224, v224\n"
"v_exp_f32 v225, v225\n"
"v_smfmac_f32_32x32x32_bf16 v[192:207], a[228:231], v[216:223], v45\n"
"v_exp_f32 v226, v226\n"
"v_exp_f32 v227, v227\n"
"v_exp_f32 v228, v228\n"
"v_mfma_f32_32x32x16_bf16 v[208:223], a[232:235], v[140:143], 0\n"
"v_exp_f32 v229, v229\n"
"v_exp_f32 v230, v230\n"
"v_exp_f32 v231, v231\n"
"v_exp_f32 v232, v232\n"
"v_exp_f32 v233, v233\n"
"v_smfmac_f32_32x32x32_bf16 v[160:175], a[224:227], v[224:231], v45\n"
"v_mov_b32 v6, 0\n"
"buffer_atomic_add v6, v5, s[40:43], s61 offen offset:256 sc0\n"
"buffer_load_dword v252, v250, s[40:43], s97 offen sc1\n"
"v_exp_f32 v234, v234\n"
"v_exp_f32 v235, v235\n"
"v_exp_f32 v236, v236\n"
"v_exp_f32 v237, v237\n"
"v_exp_f32 v238, v238\n"
"v_exp_f32 v239, v239\n"
"v_exp_f32 v208, v208\n"
"v_exp_f32 v209, v209\n"
"v_smfmac_f32_32x32x32_bf16 v[160:175], a[228:231], v[232:239], v45\n"
"v_exp_f32 v210, v210\n"
"v_exp_f32 v211, v211\n"
"v_exp_f32 v212, v212\n"
"v_mfma_f32_32x32x16_bf16 v[224:239], a[232:235], v[132:135], 0\n"
"v_exp_f32 v213, v213\n"
"v_exp_f32 v214, v214\n"
"v_exp_f32 v215, v215\n"
"v_exp_f32 v216, v216\n"
"v_exp_f32 v217, v217\n"
"v_smfmac_f32_32x32x32_bf16 v[192:207], a[236:239], v[208:215], v45\n"
"buffer_load_dwordx4 a[216:219], v2, s[40:43], s6 offen offset:2048 sc1\n"
"buffer_load_dwordx4 a[220:223], v2, s[40:43], s6 offen offset:3072 sc1\n"
"v_exp_f32 v218, v218\n"
"v_exp_f32 v219, v219\n"
"v_exp_f32 v220, v220\n"
"v_exp_f32 v221, v221\n"
"v_exp_f32 v222, v222\n"
"v_exp_f32 v223, v223\n"
"v_exp_f32 v224, v224\n"
"v_exp_f32 v225, v225\n"
"v_smfmac_f32_32x32x32_bf16 v[192:207], a[240:243], v[216:223], v45\n"
"v_exp_f32 v226, v226\n"
"v_exp_f32 v227, v227\n"
"v_exp_f32 v228, v228\n"
"v_mfma_f32_32x32x16_bf16 v[208:223], a[244:247], v[140:143], 0\n"
"v_exp_f32 v229, v229\n"
"v_exp_f32 v230, v230\n"
"v_exp_f32 v231, v231\n"
"v_exp_f32 v232, v232\n"
"v_exp_f32 v233, v233\n"
"v_smfmac_f32_32x32x32_bf16 v[160:175], a[236:239], v[224:231], v45\n"
"v_exp_f32 v234, v234\n"
"v_exp_f32 v235, v235\n"
"v_exp_f32 v236, v236\n"
"v_exp_f32 v237, v237\n"
"v_exp_f32 v238, v238\n"
"v_exp_f32 v239, v239\n"
"v_exp_f32 v208, v208\n"
"v_exp_f32 v209, v209\n"
"v_smfmac_f32_32x32x32_bf16 v[160:175], a[240:243], v[232:239], v45\n"
"v_exp_f32 v210, v210\n"
"v_exp_f32 v211, v211\n"
"v_exp_f32 v212, v212\n"
"v_mfma_f32_32x32x16_bf16 v[224:239], a[244:247], v[132:135], 0\n"
"v_exp_f32 v213, v213\n"
"v_exp_f32 v214, v214\n"
"v_exp_f32 v215, v215\n"
"v_exp_f32 v216, v216\n"
"v_exp_f32 v217, v217\n"
"v_smfmac_f32_32x32x32_bf16 v[192:207], a[248:251], v[208:215], v45\n"
"v_mov_b32 v18, 0\n"
"buffer_atomic_add v18, v5, s[40:43], s61 offen offset:256 sc0\n"
"v_exp_f32 v218, v218\n"
"v_exp_f32 v219, v219\n"
"v_exp_f32 v220, v220\n"
"v_exp_f32 v221, v221\n"
"v_exp_f32 v222, v222\n"
"v_exp_f32 v223, v223\n"
"v_exp_f32 v224, v224\n"
"v_exp_f32 v225, v225\n"
"v_smfmac_f32_32x32x32_bf16 v[192:207], a[252:255], v[216:223], v45\n"
"v_exp_f32 v226, v226\n"
"v_exp_f32 v227, v227\n"
"v_exp_f32 v228, v228\n"
"v_exp_f32 v229, v229\n"
"v_exp_f32 v230, v230\n"
"v_exp_f32 v231, v231\n"
"v_exp_f32 v232, v232\n"
"v_exp_f32 v233, v233\n"
"v_smfmac_f32_32x32x32_bf16 v[160:175], a[248:251], v[224:231], v45\n"
"v_exp_f32 v234, v234\n"
"v_exp_f32 v235, v235\n"
"v_exp_f32 v236, v236\n"
"v_exp_f32 v237, v237\n"
"v_exp_f32 v238, v238\n"
"ds_write_b128 v41, v[192:195] offset:32768\n"
"ds_write_b128 v41, v[196:199] offset:33792\n"
"ds_write_b32 v42, v200 offset:34816\n"
"v_exp_f32 v239, v239\n"
"s_nop 1\n"
"v_smfmac_f32_32x32x32_bf16 v[160:175], a[252:255], v[232:239], v45\n"
"L_Bepi:\n"
"s_cmp_lg_u32 s69, 0\n"
"s_cbranch_scc1 L_fallback\n"
"s_mov_b32 m0, s3\n"
"s_waitcnt vmcnt(4)\n"
"v_readfirstlane_b32 s57, v6\n"
"s_cmp_lg_u32 s57, 16\n"
"s_cbranch_scc1 L_p0slow\n"
"L_p0back:\n"
"buffer_load_dwordx4 v2, s[40:43], s80 offen offset:0 sc1 lds\n"
"buffer_load_dwordx4 v2, s[40:43], s80 offen offset:1024 sc1 lds\n"
"s_waitcnt lgkmcnt(0)\n"
"s_barrier\n"
"ds_read_b32 v216, v126 offset:34816\n"
"ds_read_b128 v[208:211], v43 offset:32768\n"
"ds_read_b128 v[212:215], v43 offset:33792\n"
"s_add_u32 m0, s3, 8192\n"
"s_nop 0\n"
"buffer_load_dwordx4 v2, s[40:43], s81 offen offset:0 sc1 lds\n"
"buffer_load_dwordx4 v2, s[40:43], s81 offen offset:1024 sc1 lds\n"
"s_add_u32 m0, s3, 16384\n"
"s_nop 0\n"
"buffer_load_dwordx4 v2, s[40:43], s82 offen offset:0 sc1 lds\n"
"buffer_load_dwordx4 v2, s[40:43], s82 offen offset:1024 sc1 lds\n"
"s_add_u32 m0, s3, 24576\n"
"s_nop 0\n"
"buffer_load_dwordx4 v2, s[40:43], s83 offen offset:0 sc1 lds\n"
"buffer_load_dwordx4 v2, s[40:43], s83 offen offset:1024 sc1 lds\n"
"s_waitcnt lgkmcnt(0)\n"
"v_add_f32 v168, v168, v216\n"
"v_rcp_f32 v168, v168\n"
"v_pk_add_f32 v[160:161], v[160:161], v[208:209]\n"
"v_pk_add_f32 v[162:163], v[162:163], v[210:211]\n"
"v_pk_add_f32 v[164:165], v[164:165], v[212:213]\n"
"v_pk_add_f32 v[166:167], v[166:167], v[214:215]\n"
"v_pk_mul_f32 v[160:161], v[160:161], v[168:169] op_sel_hi:[1,0]\n"
"v_pk_mul_f32 v[162:163], v[162:163], v[168:169] op_sel_hi:[1,0]\n"
"v_pk_mul_f32 v[164:165], v[164:165], v[168:169] op_sel_hi:[1,0]\n"
"v_pk_mul_f32 v[166:167], v[166:167], v[168:169] op_sel_hi:[1,0]\n"
"v_cvt_pk_bf16_f32 v208, v160, v161\n"
"v_cvt_pk_bf16_f32 v209, v162, v163\n"
"v_cvt_pk_bf16_f32 v210, v164, v165\n"
"v_cvt_pk_bf16_f32 v211, v166, v167\n"
"buffer_store_dwordx4 v[208:211], v2, s[40:43], s33 offen sc1\n"
"v_pk_add_f32 v[8:9], v[160:161], v[162:163]\n"
"v_pk_add_f32 v[8:9], v[8:9], v[164:165]\n"
"v_pk_add_f32 v[8:9], v[8:9], v[166:167]\n"
"v_fmac_f32 v8, 0x70800000, v168\n"
"v_add_f32 v8, v8, v9\n"
"v_fmac_f32 v168, 0, v8\n"
"v_cmpx_nle_f32 vcc, 0x21000000, v168\n"
"ds_write_b32 v40, v45 offset:41984\n"
"s_mov_b64 exec, -1\n"
"v_lshrrev_b32 v38, 4, v1\n"
"v_and_b32 v39, 15, v1\n"
"v_lshlrev_b32 v122, 4, v39\n"
"v_lshl_add_u32 v15, v38, 9, v122\n"
"v_lshlrev_b32 v192, 16, v112\n"
"v_and_b32 v193, 0xffff0000, v112\n"
"v_lshlrev_b32 v194, 16, v113\n"
"v_and_b32 v195, 0xffff0000, v113\n"
"v_lshlrev_b32 v196, 16, v114\n"
"v_and_b32 v197, 0xffff0000, v114\n"
"v_lshlrev_b32 v198, 16, v115\n"
"v_and_b32 v199, 0xffff0000, v115\n"
"s_waitcnt lgkmcnt(0)\n"
"s_waitcnt vmcnt(7)\n"
"s_barrier\n"
"ds_read_b32 v8, v40 offset:41984\n"
"ds_read_b128 v[160:163], v15 offset:0\n"
"ds_read_b128 v[164:167], v15 offset:256\n"
"ds_read_b128 v[168:171], v15 offset:2048\n"
"ds_read_b128 v[172:175], v15 offset:2304\n"
"ds_read_b128 v[176:179], v15 offset:4096\n"
"ds_read_b128 v[180:183], v15 offset:4352\n"
"ds_read_b128 v[184:187], v15 offset:6144\n"
"ds_read_b128 v[188:191], v15 offset:6400\n"
"v_lshl_add_u32 v16, v124, 14, v47\n"
"s_lshl2_add_u32 s60, s54, s56\n"
"s_lshl_b32 s62, s55, 7\n"
"s_add_u32 s60, s60, s62\n"
"s_lshl_b32 s64, s60, 9\n"
"s_add_u32 s64, s64, s70\n"
"s_add_u32 s64, s64, 131072\n"
"s_add_u32 s62, s56, 4\n"
"s_min_u32 s62, s62, 6\n"
"s_lshl_b32 s62, s62, 10\n"
"v_add_u32 v10, 49152, v251\n"
"v_add_u32 v12, 106496, v251\n"
"s_waitcnt lgkmcnt(8)\n"
"v_readfirstlane_b32 s57, v8\n"
"s_andn2_b32 s57, s57, s68\n"
"s_cbranch_scc1 L_fallback\n"
"s_waitcnt vmcnt(5)\n"
"s_barrier\n"
"s_waitcnt lgkmcnt(6)\n"
"v_mfma_f32_16x16x32_bf16 v[192:195], v[48:51], v[160:163], v[192:195]\n"
"ds_read_b128 v[128:131], v15 offset:8192\n"
"v_add_u32 v13, s62, v2\n"
"v_mfma_f32_16x16x32_bf16 v[196:199], v[48:51], v[164:167], v[196:199]\n"
"ds_read_b128 v[132:135], v15 offset:8448\n"
"v_add_u32 v11, 49152, v13\n"
"s_waitcnt lgkmcnt(6)\n"
"v_mfma_f32_16x16x32_bf16 v[192:195], v[52:55], v[168:171], v[192:195]\n"
"ds_read_b128 v[136:139], v15 offset:10240\n"
"v_add_u32 v13, 106496, v13\n"
"v_mfma_f32_16x16x32_bf16 v[196:199], v[52:55], v[172:175], v[196:199]\n"
"ds_read_b128 v[140:143], v15 offset:10496\n"
"s_lshr_b32 s62, s62, 4\n"
"s_waitcnt lgkmcnt(6)\n"
"v_mfma_f32_16x16x32_bf16 v[192:195], v[56:59], v[176:179], v[192:195]\n"
"ds_read_b128 v[144:147], v15 offset:12288\n"
"v_lshlrev_b32 v14, 4, v38\n"
"v_mfma_f32_16x16x32_bf16 v[196:199], v[56:59], v[180:183], v[196:199]\n"
"ds_read_b128 v[148:151], v15 offset:12544\n"
"s_lshl_b32 s60, s56, 6\n"
"s_waitcnt lgkmcnt(6)\n"
"v_mfma_f32_16x16x32_bf16 v[192:195], v[60:63], v[184:187], v[192:195]\n"
"ds_read_b128 v[152:155], v15 offset:14336\n"
"v_add_u32 v36, s60, v14\n"
"v_mfma_f32_16x16x32_bf16 v[196:199], v[60:63], v[188:191], v[196:199]\n"
"ds_read_b128 v[156:159], v15 offset:14592\n"
"v_add_u32 v37, s62, v14\n"
"s_waitcnt vmcnt(3)\n"
"s_barrier\n"
"s_waitcnt lgkmcnt(6)\n"
"v_mfma_f32_16x16x32_bf16 v[192:195], v[64:67], v[128:131], v[192:195]\n"
"ds_read_b128 v[160:163], v15 offset:16384\n"
"v_mov_b32 v40, v127\n"
"v_mfma_f32_16x16x32_bf16 v[196:199], v[64:67], v[132:135], v[196:199]\n"
"ds_read_b128 v[164:167], v15 offset:16640\n"
"v_mov_b32 v41, v127\n"
"s_waitcnt lgkmcnt(6)\n"
"v_mfma_f32_16x16x32_bf16 v[192:195], v[68:71], v[136:139], v[192:195]\n"
"ds_read_b128 v[168:171], v15 offset:18432\n"
"v_mov_b32 v42, v127\n"
"v_mfma_f32_16x16x32_bf16 v[196:199], v[68:71], v[140:143], v[196:199]\n"
"ds_read_b128 v[172:175], v15 offset:18688\n"
"v_mov_b32 v43, v127\n"
"s_waitcnt lgkmcnt(6)\n"
"v_mfma_f32_16x16x32_bf16 v[192:195], v[72:75], v[144:147], v[192:195]\n"
"ds_read_b128 v[176:179], v15 offset:20480\n"
"s_lshl_b32 s60, s54, 11\n"
"v_mfma_f32_16x16x32_bf16 v[196:199], v[72:75], v[148:151], v[196:199]\n"
"ds_read_b128 v[180:183], v15 offset:20736\n"
"v_lshl_add_u32 v253, v39, 7, s60\n"
"s_waitcnt lgkmcnt(6)\n"
"v_mfma_f32_16x16x32_bf16 v[192:195], v[76:79], v[152:155], v[192:195]\n"
"ds_read_b128 v[184:187], v15 offset:22528\n"
"s_mul_i32 s60, s54, 1536\n"
"v_mfma_f32_16x16x32_bf16 v[196:199], v[76:79], v[156:159], v[196:199]\n"
"ds_read_b128 v[188:191], v15 offset:22784\n"
"v_min_u32 v254, 11, v1\n"
"s_waitcnt vmcnt(1)\n"
"s_barrier\n"
"s_waitcnt lgkmcnt(6)\n"
"v_mfma_f32_16x16x32_bf16 v[192:195], v[80:83], v[160:163], v[192:195]\n"
"ds_read_b128 v[128:131], v15 offset:24576\n"
"v_lshl_add_u32 v254, v254, 7, s60\n"
"v_mfma_f32_16x16x32_bf16 v[196:199], v[80:83], v[164:167], v[196:199]\n"
"ds_read_b128 v[132:135], v15 offset:24832\n"
"s_xor_b32 s80, s80, 0x8000\n"
"s_waitcnt lgkmcnt(6)\n"
"v_mfma_f32_16x16x32_bf16 v[192:195], v[84:87], v[168:171], v[192:195]\n"
"ds_read_b128 v[136:139], v15 offset:26624\n"
"s_xor_b32 s81, s81, 0x8000\n"
"v_mfma_f32_16x16x32_bf16 v[196:199], v[84:87], v[172:175], v[196:199]\n"
"ds_read_b128 v[140:143], v15 offset:26880\n"
"s_xor_b32 s82, s82, 0x8000\n"
"s_waitcnt lgkmcnt(6)\n"
"v_mfma_f32_16x16x32_bf16 v[192:195], v[88:91], v[176:179], v[192:195]\n"
"ds_read_b128 v[144:147], v15 offset:28672\n"
"s_xor_b32 s83, s83, 0x8000\n"
"v_mfma_f32_16x16x32_bf16 v[196:199], v[88:91], v[180:183], v[196:199]\n"
"ds_read_b128 v[148:151], v15 offset:28928\n"
"s_waitcnt lgkmcnt(6)\n"
"v_mfma_f32_16x16x32_bf16 v[192:195], v[92:95], v[184:187], v[192:195]\n"
"ds_read_b128 v[152:155], v15 offset:30720\n"
"v_mfma_f32_16x16x32_bf16 v[196:199], v[92:95], v[188:191], v[196:199]\n"
"ds_read_b128 v[156:159], v15 offset:30976\n"
"s_waitcnt vmcnt(0)\n"
"s_barrier\n"
"buffer_atomic_add v7, v3, s[40:43], 0 offen offset:384\n"
"s_waitcnt lgkmcnt(6)\n"
"v_mfma_f32_16x16x32_bf16 v[192:195], v[96:99], v[128:131], v[192:195]\n"
"v_mfma_f32_16x16x32_bf16 v[196:199], v[96:99], v[132:135], v[196:199]\n"
"s_waitcnt lgkmcnt(4)\n"
"v_mfma_f32_16x16x32_bf16 v[192:195], v[100:103], v[136:139], v[192:195]\n"
"v_mfma_f32_16x16x32_bf16 v[196:199], v[100:103], v[140:143], v[196:199]\n"
"s_waitcnt lgkmcnt(2)\n"
"v_mfma_f32_16x16x32_bf16 v[192:195], v[104:107], v[144:147], v[192:195]\n"
"v_mfma_f32_16x16x32_bf16 v[196:199], v[104:107], v[148:151], v[196:199]\n"
"s_waitcnt lgkmcnt(0)\n"
"v_mfma_f32_16x16x32_bf16 v[192:195], v[108:111], v[152:155], v[192:195]\n"
"v_mfma_f32_16x16x32_bf16 v[196:199], v[108:111], v[156:159], v[196:199]\n"
"s_nop 6\n"
"v_cvt_pk_bf16_f32 v112, v192, v193\n"
"v_cvt_pk_bf16_f32 v114, v196, v197\n"
"v_cvt_pk_bf16_f32 v113, v194, v195\n"
"v_cvt_pk_bf16_f32 v115, v198, v199\n"
"v_mov_b32 v20, 0x3f803f80\n"
"v_permlane16_swap_b32 v112, v114\n"
"v_permlane16_swap_b32 v113, v115\n"
"v_mov_b32 v21, 0x3f803f80\n"
"buffer_store_dwordx4 v[112:115], v16, s[40:43], s64 offen sc1\n"
"s_add_u32 s64, s64, 0x8000\n"
"ds_read_b128 a[224:227], v10 offset:0\n"
"ds_read_b128 a[228:231], v11 offset:0\n"
"ds_read_b128 a[232:235], v10 offset:7168\n"
"ds_read_b128 a[236:239], v11 offset:7168\n"
"ds_read_b128 a[240:243], v10 offset:14336\n"
"ds_read_b128 a[244:247], v11 offset:14336\n"
"ds_read_b128 a[248:251], v10 offset:21504\n"
"v_mov_b32 v8, 0\n"
"buffer_atomic_add v8, v4, s[40:43], s61 offen offset:384 sc0\n"
"s_add_u32 s0, s97, 0x8000\n"
"buffer_load_dword v252, v250, s[40:43], s0 offen sc1\n"
"s_waitcnt lgkmcnt(3)\n"
"v_mfma_f32_16x16x32_bf16 v[28:31], a[224:227], v[40:43], 0\n"
"ds_read_b128 a[252:255], v11 offset:21504\n"
"v_mfma_f32_16x16x32_bf16 v[32:35], a[228:231], v[40:43], 0\n"
"ds_read_b128 a[224:227], v10 offset:28672\n"
"v_lshlrev_b32 v200, 16, v116\n"
"v_mfma_f32_16x16x32_bf16 v[28:31], a[232:235], v[40:43], v[28:31]\n"
"ds_read_b128 a[228:231], v11 offset:28672\n"
"v_and_b32 v201, 0xffff0000, v116\n"
"v_mfma_f32_16x16x32_bf16 v[32:35], a[236:239], v[40:43], v[32:35]\n"
"ds_read_b128 a[232:235], v10 offset:35840\n"
"v_lshlrev_b32 v202, 16, v117\n"
"s_waitcnt lgkmcnt(3)\n"
"v_mfma_f32_16x16x32_bf16 v[28:31], a[240:243], v[40:43], v[28:31]\n"
"ds_read_b128 a[236:239], v11 offset:35840\n"
"v_mfma_f32_16x16x32_bf16 v[32:35], a[244:247], v[40:43], v[32:35]\n"
"ds_read_b128 a[240:243], v10 offset:43008\n"
"v_and_b32 v203, 0xffff0000, v117\n"
"v_mfma_f32_16x16x32_bf16 v[28:31], a[248:251], v[40:43], v[28:31]\n"
"ds_read_b128 a[244:247], v11 offset:43008\n"
"v_lshlrev_b32 v204, 16, v118\n"
"v_mfma_f32_16x16x32_bf16 v[32:35], a[252:255], v[40:43], v[32:35]\n"
"ds_read_b128 a[248:251], v10 offset:50176\n"
"L_pad8:\n"
"s_cbranch_execz L_ret0\n"
"v_and_b32 v205, 0xffff0000, v118\n"
"s_waitcnt lgkmcnt(3)\n"
"v_mfma_f32_16x16x32_bf16 v[28:31], a[224:227], v[40:43], v[28:31]\n"
"ds_read_b128 a[252:255], v11 offset:50176\n"
"v_mfma_f32_16x16x32_bf16 v[32:35], a[228:231], v[40:43], v[32:35]\n"
"ds_read_b128 a[224:227], v12 offset:0\n"
"v_lshlrev_b32 v206, 16, v119\n"
"v_mfma_f32_16x16x32_bf16 v[28:31], a[232:235], v[40:43], v[28:31]\n"
"ds_read_b128 a[228:231], v13 offset:0\n"
"v_and_b32 v207, 0xffff0000, v119\n"
"v_mfma_f32_16x16x32_bf16 v[32:35], a[236:239], v[40:43], v[32:35]\n"
"ds_read_b128 a[232:235], v12 offset:7168\n"
"v_lshl_add_u32 v122, v121, 9, v122\n"
"s_waitcnt lgkmcnt(3)\n"
"v_mfma_f32_16x16x32_bf16 v[28:31], a[240:243], v[40:43], v[28:31]\n"
"ds_read_b128 a[236:239], v13 offset:7168\n"
"v_mfma_f32_16x16x32_bf16 v[32:35], a[244:247], v[40:43], v[32:35]\n"
"ds_read_b128 a[240:243], v12 offset:14336\n"
"v_lshl_add_u32 v45, v124, 12, v122\n"
"v_mfma_f32_16x16x32_bf16 v[28:31], a[248:251], v[40:43], v[28:31]\n"
"ds_read_b128 a[244:247], v13 offset:14336\n"
"s_lshl2_add_u32 s60, s78, s79\n"
"v_mfma_f32_16x16x32_bf16 v[32:35], a[252:255], v[40:43], v[32:35]\n"
"ds_read_b128 a[248:251], v12 offset:21504\n"
"s_lshl_b32 s62, s60, 6\n"
"s_waitcnt lgkmcnt(3)\n"
"v_mfma_f32_16x16x32_bf16 v[28:31], a[224:227], v[40:43], v[28:31]\n"
"ds_read_b128 a[252:255], v13 offset:21504\n"
"v_mfma_f32_16x16x32_bf16 v[32:35], a[228:231], v[40:43], v[32:35]\n"
"ds_read_b128 a[224:227], v12 offset:28672\n"
"v_lshl_add_u32 v46, v39, 2, s62\n"
"v_mfma_f32_16x16x32_bf16 v[28:31], a[232:235], v[40:43], v[28:31]\n"
"ds_read_b128 a[228:231], v13 offset:28672\n"
"v_lshrrev_b32 v44, 2, v39\n"
"v_mfma_f32_16x16x32_bf16 v[32:35], a[236:239], v[40:43], v[32:35]\n"
"ds_read_b128 a[232:235], v12 offset:35840\n"
"v_xor_b32 v44, v44, v38\n"
"s_waitcnt lgkmcnt(3)\n"
"v_mfma_f32_16x16x32_bf16 v[28:31], a[240:243], v[40:43], v[28:31]\n"
"ds_read_b128 a[236:239], v13 offset:35840\n"
"v_mfma_f32_16x16x32_bf16 v[32:35], a[244:247], v[40:43], v[32:35]\n"
"ds_read_b128 a[240:243], v12 offset:43008\n"
"v_lshl_add_u32 v46, v44, 10, v46\n"
"v_mfma_f32_16x16x32_bf16 v[28:31], a[248:251], v[40:43], v[28:31]\n"
"ds_read_b128 a[244:247], v13 offset:43008\n"
"s_lshl_b32 s60, s60, 14\n"
"v_mfma_f32_16x16x32_bf16 v[32:35], a[252:255], v[40:43], v[32:35]\n"
"ds_read_b128 a[248:251], v12 offset:50176\n"
"s_andn2_b32 s62, 6, s54\n"
"v_mov_b32 v9, 0\n"
"buffer_atomic_add v9, v4, s[40:43], s61 offen offset:384 sc0\n"
"s_waitcnt lgkmcnt(3)\n"
"v_mfma_f32_16x16x32_bf16 v[28:31], a[224:227], v[40:43], v[28:31]\n"
"ds_read_b128 a[252:255], v13 offset:50176\n"
"v_mfma_f32_16x16x32_bf16 v[32:35], a[228:231], v[40:43], v[32:35]\n"
"s_lshl_b32 s62, s62, 11\n"
"v_mfma_f32_16x16x32_bf16 v[28:31], a[232:235], v[40:43], v[28:31]\n"
"s_add_u32 s60, s60, s62\n"
"v_mfma_f32_16x16x32_bf16 v[32:35], a[236:239], v[40:43], v[32:35]\n"
"s_add_u32 s60, s60, s70\n"
"s_waitcnt lgkmcnt(0)\n"
"v_mfma_f32_16x16x32_bf16 v[28:31], a[240:243], v[40:43], v[28:31]\n"
"v_mfma_f32_16x16x32_bf16 v[32:35], a[244:247], v[40:43], v[32:35]\n"
"s_add_u32 s84, s60, 131072\n"
"v_mfma_f32_16x16x32_bf16 v[28:31], a[248:251], v[40:43], v[28:31]\n"
"s_xor_b32 s85, s84, 0x1000\n"
"v_mfma_f32_16x16x32_bf16 v[32:35], a[252:255], v[40:43], v[32:35]\n"
"s_xor_b32 s86, s84, 0x2000\n"
"s_xor_b32 s87, s84, 0x3000\n"
"s_lshr_b32 s60, s53, 2\n"
"s_lshl3_add_u32 s60, s78, s60\n"
"s_mul_i32 s72, s60, 28672\n"
"s_and_b32 s60, s53, 3\n"
"s_lshl2_add_u32 s60, s60, s79\n"
"s_lshl_b32 s60, s60, 8\n"
"s_add_u32 s72, s72, s60\n"
"s_mov_b32 exec_lo, 0x10001\n"
"s_mov_b32 exec_hi, 0x10001\n"
"ds_write_b128 v36, v[28:31] offset:42048\n"
"ds_write_b128 v37, v[32:35] offset:42048\n"
"s_mov_b64 exec, -1\n"
"s_waitcnt vmcnt(3)\n"
"s_barrier\n"
"buffer_atomic_add v7, v3, s[40:43], 0 offen offset:260\n"
"s_mov_b32 m0, s3\n"
"s_waitcnt lgkmcnt(0)\n"
"s_waitcnt vmcnt(3)\n"
"v_readfirstlane_b32 s57, v8\n"
"s_cmp_lg_u32 s57, s4\n"
"s_cbranch_scc1 L_p1slow\n"
"L_p1back:\n"
"s_barrier\n"
"buffer_load_dwordx4 v2, s[40:43], s80 offen offset:0 sc1 lds\n"
"buffer_load_dwordx4 v2, s[40:43], s80 offen offset:1024 sc1 lds\n"
"s_add_u32 m0, s3, 8192\n"
"s_add_u32 s62, s70, 262144\n"
"buffer_load_dwordx4 v2, s[40:43], s81 offen offset:0 sc1 lds\n"
"buffer_load_dwordx4 v2, s[40:43], s81 offen offset:1024 sc1 lds\n"
"s_add_u32 m0, s3, 16384\n"
"s_add_u32 s72, s72, s62\n"
"buffer_load_dwordx4 v2, s[40:43], s82 offen offset:0 sc1 lds\n"
"buffer_load_dwordx4 v2, s[40:43], s82 offen offset:1024 sc1 lds\n"
"s_add_u32 m0, s3, 24576\n"
"s_lshl3_add_u32 s60, s55, s56\n"
"buffer_load_dwordx4 v2, s[40:43], s83 offen offset:0 sc1 lds\n"
"buffer_load_dwordx4 v2, s[40:43], s83 offen offset:1024 sc1 lds\n"
"v_mov_b32 v6, 0\n"
"buffer_atomic_add v6, v5, s[40:43], s61 offen offset:260 sc0\n"
"s_andn2_b32 s0, s84, 0x3000\n"
"buffer_load_dword v252, v253, s[40:43], s0 offen sc1\n"
"s_mul_i32 s8, s60, 28672\n"
"s_add_u32 s8, s8, s62\n"
"s_mov_b32 s2, s8\n"
"s_mov_b32 s9, 0x3b000000\n"
"s_lshl_b32 s60, s55, 8\n"
"v_lshl_add_u32 v47, v120, 2, s60\n"
"v_lshlrev_b32 v124, 10, v120\n"
"v_lshl_add_u32 v124, v121, 4, v124\n"
"s_lshl_b32 s60, s56, 13\n"
"v_add_u32 v125, s60, v2\n"
"v_xor_b32 v126, 0x2000, v125\n"
"v_xor_b32 v127, 0x4000, v125\n"
"v_xor_b32 v12, 0x6000, v125\n"
"v_mov_b32 v13, v122\n"
"v_add_u32 v10, 49152, v2\n"
"v_add_u32 v11, 106496, v2\n"
"s_add_u32 s35, s34, 0x8000\n"
"v_and_b32 v44, 1, v1\n"
"v_mov_b64 v[24:25], 0\n"
"v_mul_u32_u24 v44, 0xaaaa, v44\n"
"v_mov_b64 v[26:27], 0\n"
"v_add_u32 v44, 0x4444, v44\n"
"v_mov_b32 v18, 0\n"
"buffer_atomic_add v18, v5, s[40:43], s61 offen offset:260 sc0\n"
"ds_read_b128 v[224:227], v14 offset:42048\n"
"ds_read_b128 v[228:231], v14 offset:42112\n"
"ds_read_b128 v[232:235], v14 offset:42176\n"
"ds_read_b128 v[236:239], v14 offset:42240\n"
"ds_read_b128 v[240:243], v14 offset:42304\n"
"ds_read_b128 v[244:247], v14 offset:42368\n"
"ds_read_b128 v[248:251], v14 offset:42432\n"
"s_waitcnt vmcnt(9)\n"
"s_barrier\n"
"ds_read_b128 v[160:163], v15 offset:0\n"
"ds_read_b128 v[164:167], v15 offset:256\n"
"ds_read_b128 v[168:171], v15 offset:2048\n"
"ds_read_b128 v[172:175], v15 offset:2304\n"
"ds_read_b128 v[176:179], v15 offset:4096\n"
"ds_read_b128 v[180:183], v15 offset:4352\n"
"ds_read_b128 v[184:187], v15 offset:6144\n"
"ds_read_b128 v[188:191], v15 offset:6400\n"
"s_waitcnt vmcnt(7)\n"
"s_barrier\n"
"s_waitcnt lgkmcnt(6)\n"
"v_mfma_f32_16x16x32_bf16 v[200:203], v[48:51], v[160:163], v[200:203]\n"
"ds_read_b128 v[128:131], v15 offset:8192\n"
"v_mfma_f32_16x16x32_bf16 v[204:207], v[48:51], v[164:167], v[204:207]\n"
"ds_read_b128 v[132:135], v15 offset:8448\n"
"s_waitcnt lgkmcnt(6)\n"
"v_mfma_f32_16x16x32_bf16 v[200:203], v[52:55], v[168:171], v[200:203]\n"
"ds_read_b128 v[136:139], v15 offset:10240\n"
"v_mfma_f32_16x16x32_bf16 v[204:207], v[52:55], v[172:175], v[204:207]\n"
"ds_read_b128 v[140:143], v15 offset:10496\n"
"s_waitcnt lgkmcnt(6)\n"
"v_mfma_f32_16x16x32_bf16 v[200:203], v[56:59], v[176:179], v[200:203]\n"
"ds_read_b128 v[144:147], v15 offset:12288\n"
"v_mfma_f32_16x16x32_bf16 v[204:207], v[56:59], v[180:183], v[204:207]\n"
"ds_read_b128 v[148:151], v15 offset:12544\n"
"s_waitcnt lgkmcnt(6)\n"
"v_mfma_f32_16x16x32_bf16 v[200:203], v[60:63], v[184:187], v[200:203]\n"
"ds_read_b128 v[152:155], v15 offset:14336\n"
"v_mfma_f32_16x16x32_bf16 v[204:207], v[60:63], v[188:191], v[204:207]\n"
"ds_read_b128 v[156:159], v15 offset:14592\n"
"s_waitcnt vmcnt(5)\n"
"s_barrier\n"
"s_waitcnt lgkmcnt(6)\n"
"v_mfma_f32_16x16x32_bf16 v[200:203], v[64:67], v[128:131], v[200:203]\n"
"ds_read_b128 v[160:163], v15 offset:16384\n"
"v_mfma_f32_16x16x32_bf16 v[204:207], v[64:67], v[132:135], v[204:207]\n"
"ds_read_b128 v[164:167], v15 offset:16640\n"
"s_waitcnt lgkmcnt(6)\n"
"v_mfma_f32_16x16x32_bf16 v[200:203], v[68:71], v[136:139], v[200:203]\n"
"ds_read_b128 v[168:171], v15 offset:18432\n"
"v_mfma_f32_16x16x32_bf16 v[204:207], v[68:71], v[140:143], v[204:207]\n"
"ds_read_b128 v[172:175], v15 offset:18688\n"
"s_waitcnt lgkmcnt(6)\n"
"v_mfma_f32_16x16x32_bf16 v[200:203], v[72:75], v[144:147], v[200:203]\n"
"ds_read_b128 v[176:179], v15 offset:20480\n"
"v_mfma_f32_16x16x32_bf16 v[204:207], v[72:75], v[148:151], v[204:207]\n"
"ds_read_b128 v[180:183], v15 offset:20736\n"
"s_waitcnt lgkmcnt(6)\n"
"v_mfma_f32_16x16x32_bf16 v[200:203], v[76:79], v[152:155], v[200:203]\n"
"ds_read_b128 v[184:187], v15 offset:22528\n"
"v_mfma_f32_16x16x32_bf16 v[204:207], v[76:79], v[156:159], v[204:207]\n"
"ds_read_b128 v[188:191], v15 offset:22784\n"
"s_waitcnt vmcnt(3)\n"
"s_barrier\n"
"s_waitcnt vmcnt(2)\n"
"v_readfirstlane_b32 s57, v6\n"
"s_cmp_lg_u32 s57, 16\n"
"s_cbranch_scc1 L_p2slow\n"
"L_p2back:\n"
"s_waitcnt lgkmcnt(6)\n"
"v_mfma_f32_16x16x32_bf16 v[200:203], v[80:83], v[160:163], v[200:203]\n"
"ds_read_b128 v[128:131], v15 offset:24576\n"
"buffer_load_dwordx4 v[48:51], v2, s[40:43], s84 offen offset:0 sc1\n"
"v_mfma_f32_16x16x32_bf16 v[204:207], v[80:83], v[164:167], v[204:207]\n"
"ds_read_b128 v[132:135], v15 offset:24832\n"
"buffer_load_dwordx4 v[52:55], v2, s[40:43], s84 offen offset:1024 sc1\n"
"s_waitcnt lgkmcnt(6)\n"
"v_mfma_f32_16x16x32_bf16 v[200:203], v[84:87], v[168:171], v[200:203]\n"
"ds_read_b128 v[136:139], v15 offset:26624\n"
"buffer_load_dwordx4 v[56:59], v2, s[40:43], s84 offen offset:2048 sc1\n"
"v_mfma_f32_16x16x32_bf16 v[204:207], v[84:87], v[172:175], v[204:207]\n"
"ds_read_b128 v[140:143], v15 offset:26880\n"
"buffer_load_dwordx4 v[60:63], v2, s[40:43], s84 offen offset:3072 sc1\n"
"s_waitcnt lgkmcnt(6)\n"
"v_mfma_f32_16x16x32_bf16 v[200:203], v[88:91], v[176:179], v[200:203]\n"
"ds_read_b128 v[144:147], v15 offset:28672\n"
"buffer_load_dwordx4 v[64:67], v2, s[40:43], s85 offen offset:0 sc1\n"
"v_mfma_f32_16x16x32_bf16 v[204:207], v[88:91], v[180:183], v[204:207]\n"
"ds_read_b128 v[148:151], v15 offset:28928\n"
"buffer_load_dwordx4 v[68:71], v2, s[40:43], s85 offen offset:1024 sc1\n"
"s_waitcnt lgkmcnt(6)\n"
"v_mfma_f32_16x16x32_bf16 v[200:203], v[92:95], v[184:187], v[200:203]\n"
"ds_read_b128 v[152:155], v15 offset:30720\n"
"v_mfma_f32_16x16x32_bf16 v[204:207], v[92:95], v[188:191], v[204:207]\n"
"ds_read_b128 v[156:159], v15 offset:30976\n"
"s_waitcnt lgkmcnt(6)\n"
"v_mfma_f32_16x16x32_bf16 v[200:203], v[96:99], v[128:131], v[200:203]\n"
"ds_read_b128 a[224:227], v10 offset:0\n"
"ds_read_b128 a[228:231], v10 offset:1024\n"
"v_mfma_f32_16x16x32_bf16 v[204:207], v[96:99], v[132:135], v[204:207]\n"
"ds_read_b128 a[232:235], v10 offset:2048\n"
"ds_read_b128 a[236:239], v10 offset:3072\n"
"s_waitcnt lgkmcnt(8)\n"
"v_mfma_f32_16x16x32_bf16 v[200:203], v[100:103], v[136:139], v[200:203]\n"
"ds_read_b128 a[240:243], v10 offset:4096\n"
"ds_read_b128 a[244:247], v10 offset:5120\n"
"v_mfma_f32_16x16x32_bf16 v[204:207], v[100:103], v[140:143], v[204:207]\n"
"ds_read_b128 a[248:251], v10 offset:6144\n"
"ds_read_b128 a[252:255], v10 offset:7168\n"
"s_waitcnt lgkmcnt(10)\n"
"v_mfma_f32_16x16x32_bf16 v[200:203], v[104:107], v[144:147], v[200:203]\n"
"ds_read_b128 v[40:43], v10 offset:8192\n"
"ds_read_b128 v[120:123], v10 offset:9216\n"
"v_mfma_f32_16x16x32_bf16 v[204:207], v[104:107], v[148:151], v[204:207]\n"
"s_waitcnt lgkmcnt(10)\n"
"v_mfma_f32_16x16x32_bf16 v[200:203], v[108:111], v[152:155], v[200:203]\n"
"v_mfma_f32_16x16x32_bf16 v[204:207], v[108:111], v[156:159], v[204:207]\n"
"s_nop 6\n"
"v_cvt_pk_bf16_f32 v116, v200, v201\n"
"v_cvt_pk_bf16_f32 v118, v204, v205\n"
"v_cvt_pk_bf16_f32 v117, v202, v203\n"
"v_cvt_pk_bf16_f32 v119, v206, v207\n"
"v_mov_b32 v22, 0x3f803f80\n"
"v_permlane16_swap_b32 v116, v118\n"
"v_permlane16_swap_b32 v117, v119\n"
"v_mov_b32 v23, 0x3f803f80\n"
"buffer_store_dwordx4 v[116:119], v16, s[40:43], s64 offen sc1\n"
"buffer_load_dwordx4 v[72:75], v2, s[40:43], s85 offen offset:2048 sc1\n"
"buffer_load_dwordx4 v[76:79], v2, s[40:43], s85 offen offset:3072 sc1\n"
"s_waitcnt vmcnt(8) lgkmcnt(7)\n"
"v_mfma_f32_16x16x32_bf16 v[28:31], v[48:51], v[48:51], 0\n"
"s_mov_b32 s66, 0xaaaaaaaa\n"
"v_mfma_f32_16x16x32_bf16 v[192:195], a[224:227], v[48:51], 0\n"
"ds_read_b128 v[220:223], v10 offset:10240\n"
"s_mov_b32 s67, 0xaaaaaaaa\n"
"s_mov_b32 s76, 0xcccccccc\n"
"v_mfma_f32_16x16x32_bf16 v[196:199], a[228:231], v[48:51], 0\n"
"ds_read_b128 a[224:227], v10 offset:11264\n"
"buffer_load_dwordx4 v[80:83], v2, s[40:43], s86 offen offset:0 sc1\n"
"s_mov_b32 s77, 0xcccccccc\n"
"v_mfma_f32_16x16x32_bf16 v[200:203], a[232:235], v[48:51], 0\n"
"ds_read_b128 a[228:231], v10 offset:12288\n"
"s_waitcnt lgkmcnt(7)\n"
"v_mfma_f32_16x16x32_bf16 v[204:207], a[236:239], v[48:51], 0\n"
"ds_read_b128 a[232:235], v10 offset:13312\n"
"v_mfma_f32_16x16x32_bf16 v[208:211], a[240:243], v[48:51], 0\n"
"ds_read_b128 a[236:239], v10 offset:14336\n"
"v_mfma_f32_16x16x32_bf16 v[212:215], a[244:247], v[48:51], 0\n"
"ds_read_b128 a[240:243], v10 offset:15360\n"
"s_waitcnt lgkmcnt(7)\n"
"v_mfma_f32_16x16x32_bf16 v[216:219], a[248:251], v[48:51], 0\n"
"ds_read_b128 a[244:247], v10 offset:16384\n"
"s_waitcnt vmcnt(8)\n"
"v_smfmac_f32_16x16x64_bf16 v[24:27], v[20:23], v[48:55], v44\n"
"v_mfma_f32_16x16x32_bf16 v[28:31], v[52:55], v[52:55], v[28:31]\n"
"v_mfma_f32_16x16x32_bf16 v[192:195], a[252:255], v[52:55], v[192:195]\n"
"ds_read_b128 a[248:251], v10 offset:17408\n"
"buffer_load_dwordx4 v[84:87], v2, s[40:43], s86 offen offset:1024 sc1\n"
"v_mfma_f32_16x16x32_bf16 v[196:199], v[40:43], v[52:55], v[196:199]\n"
"ds_read_b128 a[252:255], v10 offset:18432\n"
"s_waitcnt lgkmcnt(7)\n"
"v_mfma_f32_16x16x32_bf16 v[200:203], v[120:123], v[52:55], v[200:203]\n"
"ds_read_b128 v[40:43], v10 offset:19456\n"
"v_mfma_f32_16x16x32_bf16 v[204:207], v[220:223], v[52:55], v[204:207]\n"
"ds_read_b128 v[120:123], v10 offset:20480\n"
"v_mfma_f32_16x16x32_bf16 v[208:211], a[224:227], v[52:55], v[208:211]\n"
"ds_read_b128 v[220:223], v10 offset:21504\n"
"s_waitcnt lgkmcnt(7)\n"
"v_mfma_f32_16x16x32_bf16 v[212:215], a[228:231], v[52:55], v[212:215]\n"
"ds_read_b128 a[224:227], v10 offset:22528\n"
"v_mfma_f32_16x16x32_bf16 v[216:219], a[232:235], v[52:55], v[216:219]\n"
"ds_read_b128 a[228:231], v10 offset:23552\n"
"buffer_load_dwordx4 v[88:91], v2, s[40:43], s86 offen offset:2048 sc1\n"
"s_waitcnt vmcnt(9)\n"
"v_mfma_f32_16x16x32_bf16 v[28:31], v[56:59], v[56:59], v[28:31]\n"
"v_mfma_f32_16x16x32_bf16 v[192:195], a[236:239], v[56:59], v[192:195]\n"
"ds_read_b128 a[232:235], v10 offset:24576\n"
"s_waitcnt lgkmcnt(7)\n"
"v_mfma_f32_16x16x32_bf16 v[196:199], a[240:243], v[56:59], v[196:199]\n"
"ds_read_b128 a[236:239], v10 offset:25600\n"
"v_mfma_f32_16x16x32_bf16 v[200:203], a[244:247], v[56:59], v[200:203]\n"
"ds_read_b128 a[240:243], v10 offset:26624\n"
"v_mfma_f32_16x16x32_bf16 v[204:207], a[248:251], v[56:59], v[204:207]\n"
"ds_read_b128 a[244:247], v10 offset:27648\n"
"s_waitcnt lgkmcnt(7)\n"
"v_mfma_f32_16x16x32_bf16 v[208:211], a[252:255], v[56:59], v[208:211]\n"
"ds_read_b128 a[248:251], v10 offset:28672\n"
"v_mfma_f32_16x16x32_bf16 v[212:215], v[40:43], v[56:59], v[212:215]\n"
"ds_read_b128 a[252:255], v10 offset:29696\n"
"buffer_load_dwordx4 v[92:95], v2, s[40:43], s86 offen offset:3072 sc1\n"
"v_mfma_f32_16x16x32_bf16 v[216:219], v[120:123], v[56:59], v[216:219]\n"
"ds_read_b128 v[40:43], v10 offset:30720\n"
"s_waitcnt vmcnt(9) lgkmcnt(7)\n"
"v_smfmac_f32_16x16x64_bf16 v[24:27], v[20:23], v[56:63], v44\n"
"v_mfma_f32_16x16x32_bf16 v[28:31], v[60:63], v[60:63], v[28:31]\n"
"v_mfma_f32_16x16x32_bf16 v[192:195], v[220:223], v[60:63], v[192:195]\n"
"ds_read_b128 v[120:123], v10 offset:31744\n"
"v_mfma_f32_16x16x32_bf16 v[196:199], a[224:227], v[60:63], v[196:199]\n"
"ds_read_b128 v[220:223], v10 offset:32768\n"
"v_mfma_f32_16x16x32_bf16 v[200:203], a[228:231], v[60:63], v[200:203]\n"
"ds_read_b128 a[224:227], v10 offset:33792\n"
"s_waitcnt lgkmcnt(7)\n"
"v_mfma_f32_16x16x32_bf16 v[204:207], a[232:235], v[60:63], v[204:207]\n"
"ds_read_b128 a[228:231], v10 offset:34816\n"
"v_mfma_f32_16x16x32_bf16 v[208:211], a[236:239], v[60:63], v[208:211]\n"
"ds_read_b128 a[232:235], v10 offset:35840\n"
"buffer_load_dwordx4 v[96:99], v2, s[40:43], s87 offen offset:0 sc1\n"
"v_mfma_f32_16x16x32_bf16 v[212:215], a[240:243], v[60:63], v[212:215]\n"
"ds_read_b128 a[236:239], v10 offset:36864\n"
"s_waitcnt lgkmcnt(7)\n"
"v_mfma_f32_16x16x32_bf16 v[216:219], a[244:247], v[60:63], v[216:219]\n"
"ds_read_b128 a[240:243], v10 offset:37888\n"
"s_waitcnt vmcnt(9)\n"
"v_mfma_f32_16x16x32_bf16 v[192:195], a[248:251], v[64:67], v[192:195]\n"
"ds_read_b128 a[244:247], v10 offset:38912\n"
"v_mfma_f32_16x16x32_bf16 v[196:199], a[252:255], v[64:67], v[196:199]\n"
"ds_read_b128 a[248:251], v10 offset:39936\n"
"s_waitcnt lgkmcnt(7)\n"
"v_mfma_f32_16x16x32_bf16 v[200:203], v[40:43], v[64:67], v[200:203]\n"
"ds_read_b128 a[252:255], v10 offset:40960\n"
"v_mfma_f32_16x16x32_bf16 v[204:207], v[120:123], v[64:67], v[204:207]\n"
"ds_read_b128 v[40:43], v10 offset:41984\n"
"buffer_load_dwordx4 v[100:103], v2, s[40:43], s87 offen offset:1024 sc1\n"
"v_mfma_f32_16x16x32_bf16 v[208:211], v[220:223], v[64:67], v[208:211]\n"
"ds_read_b128 v[120:123], v10 offset:43008\n"
"s_waitcnt lgkmcnt(7)\n"
"v_mfma_f32_16x16x32_bf16 v[212:215], a[224:227], v[64:67], v[212:215]\n"
"ds_read_b128 v[220:223], v10 offset:44032\n"
"v_mfma_f32_16x16x32_bf16 v[216:219], a[228:231], v[64:67], v[216:219]\n"
"ds_read_b128 a[224:227], v10 offset:45056\n"
"s_waitcnt vmcnt(9)\n"
"v_smfmac_f32_16x16x64_bf16 v[24:27], v[20:23], v[64:71], v44\n"
"v_mfma_f32_16x16x32_bf16 v[192:195], a[232:235], v[68:71], v[192:195]\n"
"ds_read_b128 a[228:231], v10 offset:46080\n"
"s_waitcnt lgkmcnt(7)\n"
"v_mfma_f32_16x16x32_bf16 v[196:199], a[236:239], v[68:71], v[196:199]\n"
"ds_read_b128 a[232:235], v10 offset:47104\n"
"v_mfma_f32_16x16x32_bf16 v[200:203], a[240:243], v[68:71], v[200:203]\n"
"ds_read_b128 a[236:239], v10 offset:50176\n"
"buffer_load_dwordx4 v[104:107], v2, s[40:43], s87 offen offset:2048 sc1\n"
"v_mfma_f32_16x16x32_bf16 v[204:207], a[244:247], v[68:71], v[204:207]\n"
"ds_read_b128 a[240:243], v10 offset:51200\n"
"s_waitcnt lgkmcnt(7)\n"
"v_mfma_f32_16x16x32_bf16 v[208:211], a[248:251], v[68:71], v[208:211]\n"
"ds_read_b128 a[244:247], v10 offset:52224\n"
"v_mfma_f32_16x16x32_bf16 v[212:215], a[252:255], v[68:71], v[212:215]\n"
"ds_read_b128 a[248:251], v10 offset:53248\n"
"s_waitcnt vmcnt(9)\n"
"s_barrier\n"
"buffer_atomic_add v7, v3, s[40:43], 0 offen offset:388\n"
"v_mfma_f32_16x16x32_bf16 v[216:219], v[40:43], v[68:71], v[216:219]\n"
"ds_read_b128 a[252:255], v10 offset:48128\n"
"s_waitcnt vmcnt(9) lgkmcnt(7)\n"
"v_mfma_f32_16x16x32_bf16 v[192:195], v[120:123], v[72:75], v[192:195]\n"
"ds_read_b128 v[40:43], v11 offset:0\n"
"v_mfma_f32_16x16x32_bf16 v[196:199], v[220:223], v[72:75], v[196:199]\n"
"ds_read_b128 v[120:123], v11 offset:1024\n"
"buffer_load_dwordx4 v[108:111], v2, s[40:43], s87 offen offset:3072 sc1\n"
"v_mfma_f32_16x16x32_bf16 v[200:203], a[224:227], v[72:75], v[200:203]\n"
"ds_read_b128 v[220:223], v11 offset:2048\n"
"s_waitcnt lgkmcnt(7)\n"
"v_mfma_f32_16x16x32_bf16 v[204:207], a[228:231], v[72:75], v[204:207]\n"
"ds_read_b128 a[224:227], v11 offset:3072\n"
"v_mfma_f32_16x16x32_bf16 v[208:211], a[232:235], v[72:75], v[208:211]\n"
"ds_read_b128 a[228:231], v11 offset:7168\n"
"s_waitcnt vmcnt(9)\n"
"v_smfmac_f32_16x16x64_bf16 v[24:27], v[20:23], v[72:79], v44\n"
"v_mfma_f32_16x16x32_bf16 v[192:195], a[236:239], v[76:79], v[192:195]\n"
"ds_read_b128 a[232:235], v11 offset:8192\n"
"s_waitcnt lgkmcnt(7)\n"
"v_mfma_f32_16x16x32_bf16 v[196:199], a[240:243], v[76:79], v[196:199]\n"
"ds_read_b128 a[236:239], v11 offset:9216\n"
"v_mfma_f32_16x16x32_bf16 v[200:203], a[244:247], v[76:79], v[200:203]\n"
"ds_read_b128 a[240:243], v11 offset:10240\n"
"v_mfma_f32_16x16x32_bf16 v[204:207], a[248:251], v[76:79], v[204:207]\n"
"ds_read_b128 a[244:247], v11 offset:14336\n"
"s_waitcnt lgkmcnt(7)\n"
"v_mfma_f32_16x16x32_bf16 v[212:215], a[252:255], v[72:75], v[212:215]\n"
"ds_read_b128 a[248:251], v11 offset:15360\n"
"s_waitcnt vmcnt(8)\n"
"v_mfma_f32_16x16x32_bf16 v[192:195], v[40:43], v[80:83], v[192:195]\n"
"ds_read_b128 a[252:255], v11 offset:16384\n"
"v_mfma_f32_16x16x32_bf16 v[196:199], v[120:123], v[80:83], v[196:199]\n"
"ds_read_b128 v[40:43], v11 offset:17408\n"
"s_waitcnt lgkmcnt(7)\n"
"v_mfma_f32_16x16x32_bf16 v[200:203], v[220:223], v[80:83], v[200:203]\n"
"ds_read_b128 v[120:123], v11 offset:21504\n"
"v_mfma_f32_16x16x32_bf16 v[204:207], a[224:227], v[80:83], v[204:207]\n"
"ds_read_b128 v[220:223], v11 offset:22528\n"
"s_waitcnt vmcnt(7)\n"
"v_smfmac_f32_16x16x64_bf16 v[24:27], v[20:23], v[80:87], v44\n"
"v_mfma_f32_16x16x32_bf16 v[192:195], a[228:231], v[84:87], v[192:195]\n"
"ds_read_b128 a[224:227], v11 offset:23552\n"
"s_waitcnt lgkmcnt(7)\n"
"v_mfma_f32_16x16x32_bf16 v[196:199], a[232:235], v[84:87], v[196:199]\n"
"ds_read_b128 a[228:231], v11 offset:24576\n"
"v_mfma_f32_16x16x32_bf16 v[200:203], a[236:239], v[84:87], v[200:203]\n"
"ds_read_b128 a[232:235], v11 offset:28672\n"
"v_mfma_f32_16x16x32_bf16 v[204:207], a[240:243], v[84:87], v[204:207]\n"
"ds_read_b128 a[236:239], v11 offset:29696\n"
"s_waitcnt vmcnt(6) lgkmcnt(7)\n"
"v_mfma_f32_16x16x32_bf16 v[192:195], a[244:247], v[88:91], v[192:195]\n"
"ds_read_b128 a[240:243], v11 offset:30720\n"
"v_mfma_f32_16x16x32_bf16 v[196:199], a[248:251], v[88:91], v[196:199]\n"
"ds_read_b128 a[244:247], v11 offset:31744\n"
"v_mfma_f32_16x16x32_bf16 v[200:203], a[252:255], v[88:91], v[200:203]\n"
"ds_read_b128 a[248:251], v11 offset:35840\n"
"s_waitcnt lgkmcnt(7)\n"
"v_mfma_f32_16x16x32_bf16 v[204:207], v[40:43], v[88:91], v[204:207]\n"
"ds_read_b128 a[252:255], v11 offset:36864\n"
"s_waitcnt vmcnt(5)\n"
"v_smfmac_f32_16x16x64_bf16 v[24:27], v[20:23], v[88:95], v44\n"
"v_mfma_f32_16x16x32_bf16 v[192:195], v[120:123], v[92:95], v[192:195]\n"
"ds_read_b128 v[40:43], v11 offset:37888\n"
"v_mfma_f32_16x16x32_bf16 v[196:199], v[220:223], v[92:95], v[196:199]\n"
"ds_read_b128 v[120:123], v11 offset:38912\n"
"s_waitcnt lgkmcnt(7)\n"
"v_mfma_f32_16x16x32_bf16 v[200:203], a[224:227], v[92:95], v[200:203]\n"
"ds_read_b128 v[220:223], v11 offset:43008\n"
"v_mfma_f32_16x16x32_bf16 v[204:207], a[228:231], v[92:95], v[204:207]\n"
"ds_read_b128 a[224:227], v11 offset:44032\n"
"s_waitcnt vmcnt(4)\n"
"v_mfma_f32_16x16x32_bf16 v[192:195], a[232:235], v[96:99], v[192:195]\n"
"ds_read_b128 a[228:231], v11 offset:45056\n"
"v_mov_b32 v8, 0\n"
"buffer_atomic_add v8, v5, s[40:43], s61 offen offset:388 sc0\n"
"s_andn2_b32 s0, s84, 0x3000\n"
"s_add_u32 s0, s0, 0x8000\n"
"buffer_load_dword v252, v253, s[40:43], s0 offen sc1\n"
"s_waitcnt lgkmcnt(7)\n"
"v_mfma_f32_16x16x32_bf16 v[196:199], a[236:239], v[96:99], v[196:199]\n"
"ds_read_b128 a[232:235], v11 offset:46080\n"
"v_mfma_f32_16x16x32_bf16 v[200:203], a[240:243], v[96:99], v[200:203]\n"
"ds_read_b128 a[236:239], v11 offset:50176\n"
"v_mfma_f32_16x16x32_bf16 v[204:207], a[244:247], v[96:99], v[204:207]\n"
"ds_read_b128 a[240:243], v11 offset:51200\n"
"s_waitcnt vmcnt(5) lgkmcnt(7)\n"
"v_smfmac_f32_16x16x64_bf16 v[24:27], v[20:23], v[96:103], v44\n"
"v_mfma_f32_16x16x32_bf16 v[192:195], a[248:251], v[100:103], v[192:195]\n"
"ds_read_b128 a[244:247], v11 offset:52224\n"
"v_mfma_f32_16x16x32_bf16 v[196:199], a[252:255], v[100:103], v[196:199]\n"
"ds_read_b128 a[248:251], v11 offset:53248\n"
"v_mfma_f32_16x16x32_bf16 v[200:203], v[40:43], v[100:103], v[200:203]\n"
"ds_read_b128 a[252:255], v10 offset:49152\n"
"s_waitcnt lgkmcnt(7)\n"
"v_mfma_f32_16x16x32_bf16 v[204:207], v[120:123], v[100:103], v[204:207]\n"
"ds_read_b128 v[40:43], v10 offset:54272\n"
"s_waitcnt vmcnt(4)\n"
"v_mfma_f32_16x16x32_bf16 v[192:195], v[220:223], v[104:107], v[192:195]\n"
"ds_read_b128 v[120:123], v10 offset:55296\n"
"v_mfma_f32_16x16x32_bf16 v[196:199], a[224:227], v[104:107], v[196:199]\n"
"ds_read_b128 v[220:223], v10 offset:56320\n"
"s_waitcnt lgkmcnt(7)\n"
"v_mfma_f32_16x16x32_bf16 v[200:203], a[228:231], v[104:107], v[200:203]\n"
"ds_read_b128 a[224:227], v11 offset:4096\n"
"v_mfma_f32_16x16x32_bf16 v[204:207], a[232:235], v[104:107], v[204:207]\n"
"ds_read_b128 a[228:231], v11 offset:5120\n"
"s_waitcnt vmcnt(2)\n"
"v_smfmac_f32_16x16x64_bf16 v[24:27], v[20:23], v[104:111], v44\n"
"v_mfma_f32_16x16x32_bf16 v[192:195], a[236:239], v[108:111], v[192:195]\n"
"ds_read_b128 a[232:235], v11 offset:6144\n"
"s_waitcnt lgkmcnt(7)\n"
"v_mfma_f32_16x16x32_bf16 v[196:199], a[240:243], v[108:111], v[196:199]\n"
"ds_read_b128 a[236:239], v11 offset:11264\n"
"v_mfma_f32_16x16x32_bf16 v[200:203], a[244:247], v[108:111], v[200:203]\n"
"ds_read_b128 a[240:243], v11 offset:12288\n"
"s_nop 0\n"
"v_add_f32 v24, v24, v25\n"
"v_mul_f32 v36, 0xbb000000, v24\n"
"v_pk_fma_f32 v[192:193], v[224:225], v[36:37], v[192:193] op_sel_hi:[1,0,1]\n"
"v_pk_fma_f32 v[194:195], v[226:227], v[36:37], v[194:195] op_sel_hi:[1,0,1]\n"
"v_pk_fma_f32 v[196:197], v[228:229], v[36:37], v[196:197] op_sel_hi:[1,0,1]\n"
"v_pk_fma_f32 v[198:199], v[230:231], v[36:37], v[198:199] op_sel_hi:[1,0,1]\n"
"v_cvt_pk_bf16_f32 v192, v192, v193\n"
"v_cvt_pk_bf16_f32 v193, v194, v195\n"
"v_cvt_pk_bf16_f32 v194, v196, v197\n"
"v_cvt_pk_bf16_f32 v195, v198, v199\n"
"v_pk_max_i16 v194, v194, 0\n"
"v_pk_max_i16 v192, v192, 0\n"
"v_pk_max_i16 v195, v195, 0\n"
"v_pk_max_i16 v193, v193, 0\n"
"v_permlane16_swap_b32 v192, v194\n"
"s_nop 0\n"
"v_permlane16_swap_b32 v193, v195\n"
"buffer_store_dwordx4 v[192:195], v45, s[40:43], s72 offen sc1\n"
"v_mfma_f32_16x16x32_bf16 v[204:207], a[248:251], v[108:111], v[204:207]\n"
"ds_read_b128 a[244:247], v11 offset:13312\n"
"s_waitcnt lgkmcnt(7)\n"
"v_mfma_f32_16x16x32_bf16 v[216:219], a[252:255], v[72:75], v[216:219]\n"
"ds_read_b128 a[248:251], v11 offset:18432\n"
"v_mov_b32 v9, 0\n"
"buffer_atomic_add v9, v5, s[40:43], s61 offen offset:388 sc0\n"
"v_pk_fma_f32 v[200:201], v[232:233], v[36:37], v[200:201] op_sel_hi:[1,0,1]\n"
"v_pk_fma_f32 v[202:203], v[234:235], v[36:37], v[202:203] op_sel_hi:[1,0,1]\n"
"v_pk_fma_f32 v[204:205], v[236:237], v[36:37], v[204:205] op_sel_hi:[1,0,1]\n"
"v_pk_fma_f32 v[206:207], v[238:239], v[36:37], v[206:207] op_sel_hi:[1,0,1]\n"
"v_cvt_pk_bf16_f32 v200, v200, v201\n"
"v_cvt_pk_bf16_f32 v201, v202, v203\n"
"v_cvt_pk_bf16_f32 v202, v204, v205\n"
"v_cvt_pk_bf16_f32 v203, v206, v207\n"
"v_pk_max_i16 v202, v202, 0\n"
"v_pk_max_i16 v200, v200, 0\n"
"v_pk_max_i16 v203, v203, 0\n"
"v_pk_max_i16 v201, v201, 0\n"
"s_add_u32 s72, s72, 8192\n"
"v_permlane16_swap_b32 v200, v202\n"
"v_permlane16_swap_b32 v201, v203\n"
"buffer_store_dwordx4 v[200:203], v45, s[40:43], s72 offen sc1\n"
"v_mfma_f32_16x16x32_bf16 v[208:211], v[40:43], v[76:79], v[208:211]\n"
"ds_read_b128 a[252:255], v11 offset:19456\n"
"v_mfma_f32_16x16x32_bf16 v[212:215], v[120:123], v[76:79], v[212:215]\n"
"ds_read_b128 v[40:43], v11 offset:20480\n"
"s_waitcnt lgkmcnt(7)\n"
"v_mfma_f32_16x16x32_bf16 v[216:219], v[220:223], v[76:79], v[216:219]\n"
"ds_read_b128 v[120:123], v11 offset:25600\n"
"v_mfma_f32_16x16x32_bf16 v[208:211], a[224:227], v[80:83], v[208:211]\n"
"ds_read_b128 v[220:223], v11 offset:26624\n"
"v_mfma_f32_16x16x32_bf16 v[212:215], a[228:231], v[80:83], v[212:215]\n"
"ds_read_b128 a[224:227], v11 offset:27648\n"
"s_waitcnt lgkmcnt(7)\n"
"v_mfma_f32_16x16x32_bf16 v[216:219], a[232:235], v[80:83], v[216:219]\n"
"ds_read_b128 a[228:231], v11 offset:32768\n"
"v_mfma_f32_16x16x32_bf16 v[208:211], a[236:239], v[84:87], v[208:211]\n"
"ds_read_b128 a[232:235], v11 offset:33792\n"
"v_mfma_f32_16x16x32_bf16 v[212:215], a[240:243], v[84:87], v[212:215]\n"
"ds_read_b128 a[236:239], v11 offset:34816\n"
"s_waitcnt lgkmcnt(7)\n"
"v_mfma_f32_16x16x32_bf16 v[216:219], a[244:247], v[84:87], v[216:219]\n"
"ds_read_b128 a[240:243], v11 offset:39936\n"
"v_mfma_f32_16x16x32_bf16 v[208:211], a[248:251], v[88:91], v[208:211]\n"
"ds_read_b128 a[244:247], v11 offset:40960\n"
"v_mfma_f32_16x16x32_bf16 v[212:215], a[252:255], v[88:91], v[212:215]\n"
"ds_read_b128 a[248:251], v11 offset:41984\n"
"s_waitcnt lgkmcnt(7)\n"
"v_mfma_f32_16x16x32_bf16 v[216:219], v[40:43], v[88:91], v[216:219]\n"
"ds_read_b128 a[252:255], v11 offset:47104\n"
"v_mfma_f32_16x16x32_bf16 v[208:211], v[120:123], v[92:95], v[208:211]\n"
"ds_read_b128 v[40:43], v11 offset:48128\n"
"v_mfma_f32_16x16x32_bf16 v[212:215], v[220:223], v[92:95], v[212:215]\n"
"ds_read_b128 v[120:123], v11 offset:49152\n"
"s_waitcnt lgkmcnt(7)\n"
"v_mfma_f32_16x16x32_bf16 v[216:219], a[224:227], v[92:95], v[216:219]\n"
"ds_read_b128 v[220:223], v11 offset:54272\n"
"s_xor_b32 s84, s84, 0x8000\n"
"s_xor_b32 s85, s85, 0x8000\n"
"s_xor_b32 s86, s86, 0x8000\n"
"v_mfma_f32_16x16x32_bf16 v[208:211], a[228:231], v[96:99], v[208:211]\n"
"L_pad9:\n"
"s_cbranch_execz L_ret0\n"
"ds_read_b128 a[224:227], v11 offset:55296\n"
"s_xor_b32 s87, s87, 0x8000\n"
"v_mov_b32 v24, 0\n"
"v_mfma_f32_16x16x32_bf16 v[212:215], a[232:235], v[96:99], v[212:215]\n"
"ds_read_b128 a[228:231], v11 offset:56320\n"
"v_mov_b32 v25, 0\n"
"s_waitcnt lgkmcnt(7)\n"
"v_mfma_f32_16x16x32_bf16 v[216:219], a[236:239], v[96:99], v[216:219]\n"
"ds_read_b128 a[232:235], v10 offset:0\n"
"v_mfma_f32_16x16x32_bf16 v[208:211], a[240:243], v[100:103], v[208:211]\n"
"ds_read_b128 a[236:239], v10 offset:1024\n"
"v_mfma_f32_16x16x32_bf16 v[212:215], a[244:247], v[100:103], v[212:215]\n"
"ds_read_b128 a[240:243], v10 offset:2048\n"
"s_waitcnt vmcnt(4)\n"
"v_readfirstlane_b32 s57, v8\n"
"s_cmp_lg_u32 s57, 16\n"
"s_cbranch_scc1 L_p3slow\n"
"L_p3back:\n"
"s_waitcnt lgkmcnt(7)\n"
"v_mfma_f32_16x16x32_bf16 v[216:219], a[248:251], v[100:103], v[216:219]\n"
"ds_read_b128 a[244:247], v10 offset:3072\n"
"buffer_load_dwordx4 v[128:131], v2, s[40:43], s84 offen offset:0 sc1\n"
"v_mfma_f32_16x16x32_bf16 v[208:211], a[252:255], v[104:107], v[208:211]\n"
"ds_read_b128 a[248:251], v10 offset:4096\n"
"buffer_load_dwordx4 v[132:135], v2, s[40:43], s84 offen offset:1024 sc1\n"
"v_mfma_f32_16x16x32_bf16 v[212:215], v[40:43], v[104:107], v[212:215]\n"
"ds_read_b128 a[252:255], v10 offset:5120\n"
"buffer_load_dwordx4 v[136:139], v2, s[40:43], s84 offen offset:2048 sc1\n"
"s_waitcnt lgkmcnt(7)\n"
"v_mfma_f32_16x16x32_bf16 v[216:219], v[120:123], v[104:107], v[216:219]\n"
"ds_read_b128 v[40:43], v10 offset:7168\n"
"buffer_load_dwordx4 v[140:143], v2, s[40:43], s84 offen offset:3072 sc1\n"
"v_mfma_f32_16x16x32_bf16 v[208:211], v[220:223], v[108:111], v[208:211]\n"
"ds_read_b128 v[120:123], v10 offset:8192\n"
"buffer_load_dwordx4 v[144:147], v2, s[40:43], s85 offen offset:0 sc1\n"
"v_mfma_f32_16x16x32_bf16 v[212:215], a[224:227], v[108:111], v[212:215]\n"
"ds_read_b128 v[220:223], v10 offset:9216\n"
"buffer_load_dwordx4 v[148:151], v2, s[40:43], s85 offen offset:1024 sc1\n"
"s_waitcnt lgkmcnt(7)\n"
"v_mfma_f32_16x16x32_bf16 v[216:219], a[228:231], v[108:111], v[216:219]\n"
"ds_read_b128 a[224:227], v10 offset:10240\n"
"buffer_load_dwordx4 v[152:155], v2, s[40:43], s85 offen offset:2048 sc1\n"
"v_mfma_f32_16x16x32_bf16 v[28:31], v[64:67], v[64:67], v[28:31]\n"
"v_mfma_f32_16x16x32_bf16 v[28:31], v[68:71], v[68:71], v[28:31]\n"
"v_fmac_f32 v208, v36, v240\n"
"v_fmac_f32 v209, v36, v241\n"
"v_fmac_f32 v210, v36, v242\n"
"v_mfma_f32_16x16x32_bf16 v[28:31], v[72:75], v[72:75], v[28:31]\n"
"v_fmac_f32 v211, v36, v243\n"
"v_fmac_f32 v212, v36, v244\n"
"v_fmac_f32 v213, v36, v245\n"
"v_mfma_f32_16x16x32_bf16 v[28:31], v[76:79], v[76:79], v[28:31]\n"
"v_fmac_f32 v214, v36, v246\n"
"v_fmac_f32 v215, v36, v247\n"
"v_cvt_pk_bf16_f32 v208, v208, v209\n"
"v_mfma_f32_16x16x32_bf16 v[28:31], v[80:83], v[80:83], v[28:31]\n"
"v_cvt_pk_bf16_f32 v209, v210, v211\n"
"v_cvt_pk_bf16_f32 v210, v212, v213\n"
"v_cvt_pk_bf16_f32 v211, v214, v215\n"
"v_mfma_f32_16x16x32_bf16 v[28:31], v[84:87], v[84:87], v[28:31]\n"
"v_pk_max_i16 v210, v210, 0\n"
"v_pk_max_i16 v208, v208, 0\n"
"v_pk_max_i16 v211, v211, 0\n"
"v_mfma_f32_16x16x32_bf16 v[28:31], v[88:91], v[88:91], v[28:31]\n"
"v_pk_max_i16 v209, v209, 0\n"
"v_permlane16_swap_b32 v208, v210\n"
"s_nop 0\n"
"v_permlane16_swap_b32 v209, v211\n"
"v_mfma_f32_16x16x32_bf16 v[28:31], v[92:95], v[92:95], v[28:31]\n"
"v_fmac_f32 v216, v36, v248\n"
"v_fmac_f32 v217, v36, v249\n"
"v_fmac_f32 v218, v36, v250\n"
"v_mfma_f32_16x16x32_bf16 v[28:31], v[96:99], v[96:99], v[28:31]\n"
"v_fmac_f32 v219, v36, v251\n"
"v_cvt_pk_bf16_f32 v216, v216, v217\n"
"v_cvt_pk_bf16_f32 v217, v218, v219\n"
"v_mfma_f32_16x16x32_bf16 v[28:31], v[100:103], v[100:103], v[28:31]\n"
"v_pk_max_i16 v218, v216, 0\n"
"v_pk_max_i16 v216, v216, 0\n"
"v_pk_max_i16 v219, v217, 0\n"
"v_mfma_f32_16x16x32_bf16 v[28:31], v[104:107], v[104:107], v[28:31]\n"
"v_pk_max_i16 v217, v217, 0\n"
"v_permlane16_swap_b32 v216, v218\n"
"s_nop 0\n"
"v_permlane16_swap_b32 v217, v219\n"
"v_mfma_f32_16x16x32_bf16 v[28:31], v[108:111], v[108:111], v[28:31]\n"
"s_waitcnt vmcnt(7)\n"
"s_barrier\n"
"buffer_atomic_add v7, v3, s[40:43], 0 offen offset:264\n"
"s_waitcnt vmcnt(7)\n"
"v_mfma_f32_16x16x32_bf16 v[32:35], v[128:131], v[128:131], 0\n"
"v_mfma_f32_16x16x32_bf16 v[48:51], a[232:235], v[128:131], 0\n"
"ds_read_b128 a[228:231], v10 offset:6144\n"
"v_mfma_f32_16x16x32_bf16 v[52:55], a[236:239], v[128:131], 0\n"
"ds_read_b128 a[232:235], v10 offset:11264\n"
"s_waitcnt lgkmcnt(7)\n"
"v_mfma_f32_16x16x32_bf16 v[56:59], a[240:243], v[128:131], 0\n"
"ds_read_b128 a[236:239], v10 offset:14336\n"
"buffer_load_dwordx4 v[156:159], v2, s[40:43], s85 offen offset:3072 sc1\n"
"v_mfma_f32_16x16x32_bf16 v[60:63], a[244:247], v[128:131], 0\n"
"ds_read_b128 a[240:243], v10 offset:15360\n"
"v_mfma_f32_16x16x32_bf16 v[64:67], a[248:251], v[128:131], 0\n"
"ds_read_b128 a[244:247], v10 offset:16384\n"
"v_cndmask_b32 v28, v28, v29, s[66:67]\n"
"v_cndmask_b32 v30, v30, v31, s[66:67]\n"
"s_waitcnt lgkmcnt(7)\n"
"v_mfma_f32_16x16x32_bf16 v[68:71], a[252:255], v[128:131], 0\n"
"ds_read_b128 a[248:251], v10 offset:17408\n"
"v_mul_f32 v29, v36, v36\n"
"v_cndmask_b32 v28, v28, v30, s[76:77]\n"
"v_fma_f32 v28, v28, s9, -v29\n"
"s_waitcnt vmcnt(7)\n"
"v_smfmac_f32_16x16x64_bf16 v[24:27], v[20:23], v[128:135], v44\n"
"v_max_f32 v28, 0, v28\n"
"v_mfma_f32_16x16x32_bf16 v[32:35], v[132:135], v[132:135], v[32:35]\n"
"v_add_f32 v28, 0x3727c5ac, v28\n"
"v_mfma_f32_16x16x32_bf16 v[48:51], v[40:43], v[132:135], v[48:51]\n"
"ds_read_b128 a[252:255], v10 offset:12288\n"
"buffer_load_dwordx4 v[160:163], v2, s[40:43], s86 offen offset:0 sc1\n"
"v_rsq_f32 v28, v28\n"
"v_mfma_f32_16x16x32_bf16 v[52:55], v[120:123], v[132:135], v[52:55]\n"
"ds_read_b128 v[40:43], v10 offset:13312\n"
"ds_write_b32 v46, v28 offset:42752\n"
"s_waitcnt lgkmcnt(8)\n"
"v_mfma_f32_16x16x32_bf16 v[56:59], v[220:223], v[132:135], v[56:59]\n"
"ds_read_b128 v[120:123], v10 offset:21504\n"
"v_mfma_f32_16x16x32_bf16 v[60:63], a[224:227], v[132:135], v[60:63]\n"
"ds_read_b128 v[220:223], v10 offset:22528\n"
"v_mfma_f32_16x16x32_bf16 v[72:75], a[228:231], v[128:131], 0\n"
"ds_read_b128 a[224:227], v10 offset:23552\n"
"buffer_load_dwordx4 v[164:167], v2, s[40:43], s86 offen offset:1024 sc1\n"
"s_waitcnt lgkmcnt(8)\n"
"v_mfma_f32_16x16x32_bf16 v[64:67], a[232:235], v[132:135], v[64:67]\n"
"ds_read_b128 a[228:231], v10 offset:24576\n"
"s_waitcnt vmcnt(8)\n"
"v_mfma_f32_16x16x32_bf16 v[32:35], v[136:139], v[136:139], v[32:35]\n"
"v_mfma_f32_16x16x32_bf16 v[48:51], a[236:239], v[136:139], v[48:51]\n"
"ds_read_b128 a[232:235], v10 offset:18432\n"
"v_mfma_f32_16x16x32_bf16 v[52:55], a[240:243], v[136:139], v[52:55]\n"
"ds_read_b128 a[236:239], v10 offset:19456\n"
"s_waitcnt lgkmcnt(8)\n"
"v_mfma_f32_16x16x32_bf16 v[56:59], a[244:247], v[136:139], v[56:59]\n"
"ds_read_b128 a[240:243], v10 offset:28672\n"
"buffer_load_dwordx4 v[168:171], v2, s[40:43], s86 offen offset:2048 sc1\n"
"v_mfma_f32_16x16x32_bf16 v[60:63], a[248:251], v[136:139], v[60:63]\n"
"ds_read_b128 a[244:247], v10 offset:29696\n"
"v_mfma_f32_16x16x32_bf16 v[68:71], a[252:255], v[132:135], v[68:71]\n"
"ds_read_b128 a[248:251], v10 offset:30720\n"
"v_mov_b32 v6, 0\n"
"buffer_atomic_add v6, v5, s[40:43], s61 offen offset:264 sc0\n"
"s_waitcnt lgkmcnt(7)\n"
"v_mfma_f32_16x16x32_bf16 v[72:75], v[40:43], v[132:135], v[72:75]\n"
"ds_read_b128 a[252:255], v10 offset:31744\n"
"s_waitcnt vmcnt(9)\n"
"v_smfmac_f32_16x16x64_bf16 v[24:27], v[20:23], v[136:143], v44\n"
"v_mfma_f32_16x16x32_bf16 v[32:35], v[140:143], v[140:143], v[32:35]\n"
"v_mfma_f32_16x16x32_bf16 v[48:51], v[120:123], v[140:143], v[48:51]\n"
"ds_read_b128 v[40:43], v10 offset:20480\n"
"buffer_load_dwordx4 v[172:175], v2, s[40:43], s86 offen offset:3072 sc1\n"
"v_mfma_f32_16x16x32_bf16 v[52:55], v[220:223], v[140:143], v[52:55]\n"
"ds_read_b128 v[120:123], v10 offset:25600\n"
"s_waitcnt lgkmcnt(7)\n"
"v_mfma_f32_16x16x32_bf16 v[56:59], a[224:227], v[140:143], v[56:59]\n"
"ds_read_b128 v[220:223], v10 offset:35840\n"
"v_mfma_f32_16x16x32_bf16 v[60:63], a[228:231], v[140:143], v[60:63]\n"
"ds_read_b128 a[224:227], v10 offset:36864\n"
"v_mfma_f32_16x16x32_bf16 v[64:67], a[232:235], v[136:139], v[64:67]\n"
"ds_read_b128 a[228:231], v10 offset:37888\n"
"buffer_load_dwordx4 v[176:179], v2, s[40:43], s87 offen offset:0 sc1\n"
"s_waitcnt lgkmcnt(7)\n"
"v_mfma_f32_16x16x32_bf16 v[68:71], a[236:239], v[136:139], v[68:71]\n"
"ds_read_b128 a[232:235], v10 offset:38912\n"
"s_waitcnt vmcnt(10)\n"
"v_mfma_f32_16x16x32_bf16 v[48:51], a[240:243], v[144:147], v[48:51]\n"
"ds_read_b128 a[236:239], v10 offset:26624\n"
"v_mfma_f32_16x16x32_bf16 v[52:55], a[244:247], v[144:147], v[52:55]\n"
"ds_read_b128 a[240:243], v10 offset:43008\n"
"s_waitcnt lgkmcnt(7)\n"
"v_mfma_f32_16x16x32_bf16 v[56:59], a[248:251], v[144:147], v[56:59]\n"
"ds_read_b128 a[244:247], v10 offset:44032\n"
"buffer_load_dwordx4 v[180:183], v2, s[40:43], s87 offen offset:1024 sc1\n"
"v_mfma_f32_16x16x32_bf16 v[60:63], a[252:255], v[144:147], v[60:63]\n"
"ds_read_b128 a[248:251], v10 offset:45056\n"
"v_mfma_f32_16x16x32_bf16 v[72:75], v[40:43], v[136:139], v[72:75]\n"
"ds_read_b128 a[252:255], v10 offset:46080\n"
"s_waitcnt lgkmcnt(7)\n"
"v_mfma_f32_16x16x32_bf16 v[64:67], v[120:123], v[140:143], v[64:67]\n"
"ds_read_b128 v[40:43], v10 offset:27648\n"
"s_waitcnt vmcnt(10)\n"
"v_smfmac_f32_16x16x64_bf16 v[24:27], v[20:23], v[144:151], v44\n"
"v_mfma_f32_16x16x32_bf16 v[48:51], v[220:223], v[148:151], v[48:51]\n"
"ds_read_b128 v[120:123], v10 offset:50176\n"
"buffer_load_dwordx4 v[184:187], v2, s[40:43], s87 offen offset:2048 sc1\n"
"v_mfma_f32_16x16x32_bf16 v[52:55], a[224:227], v[148:151], v[52:55]\n"
"ds_read_b128 v[220:223], v10 offset:51200\n"
"s_waitcnt lgkmcnt(7)\n"
"v_mfma_f32_16x16x32_bf16 v[56:59], a[228:231], v[148:151], v[56:59]\n"
"ds_read_b128 a[224:227], v10 offset:52224\n"
"v_mfma_f32_16x16x32_bf16 v[60:63], a[232:235], v[148:151], v[60:63]\n"
"ds_read_b128 a[228:231], v10 offset:53248\n"
"v_mfma_f32_16x16x32_bf16 v[68:71], a[236:239], v[140:143], v[68:71]\n"
"ds_read_b128 a[232:235], v10 offset:32768\n"
"v_mov_b32 v18, 0\n"
"buffer_atomic_add v18, v5, s[40:43], s61 offen offset:264 sc0\n"
"s_waitcnt vmcnt(11) lgkmcnt(7)\n"
"v_mfma_f32_16x16x32_bf16 v[48:51], a[240:243], v[152:155], v[48:51]\n"
"ds_read_b128 a[236:239], v11 offset:0\n"
"buffer_load_dwordx4 v[188:191], v2, s[40:43], s87 offen offset:3072 sc1\n"
"v_mfma_f32_16x16x32_bf16 v[52:55], a[244:247], v[152:155], v[52:55]\n"
"ds_read_b128 a[240:243], v11 offset:1024\n"
"s_add_u32 s72, s72, 8192\n"
"buffer_store_dwordx4 v[208:211], v45, s[40:43], s72 offen sc1\n"
"v_mfma_f32_16x16x32_bf16 v[56:59], a[248:251], v[152:155], v[56:59]\n"
"ds_read_b128 a[244:247], v11 offset:2048\n"
"s_waitcnt lgkmcnt(7)\n"
"v_mfma_f32_16x16x32_bf16 v[60:63], a[252:255], v[152:155], v[60:63]\n"
"ds_read_b128 a[248:251], v11 offset:3072\n"
"s_add_u32 s72, s72, 8192\n"
"buffer_store_dwordx4 v[216:219], v13, s[40:43], s72 offen sc1\n"
"v_mfma_f32_16x16x32_bf16 v[72:75], v[40:43], v[140:143], v[72:75]\n"
"ds_read_b128 a[252:255], v10 offset:33792\n"
"s_waitcnt vmcnt(12)\n"
"v_smfmac_f32_16x16x64_bf16 v[24:27], v[20:23], v[152:159], v44\n"
"v_mfma_f32_16x16x32_bf16 v[48:51], v[120:123], v[156:159], v[48:51]\n"
"ds_read_b128 v[40:43], v11 offset:7168\n"
"s_waitcnt lgkmcnt(7)\n"
"v_mfma_f32_16x16x32_bf16 v[52:55], v[220:223], v[156:159], v[52:55]\n"
"ds_read_b128 v[120:123], v11 offset:8192\n"
"v_mfma_f32_16x16x32_bf16 v[56:59], a[224:227], v[156:159], v[56:59]\n"
"ds_read_b128 v[220:223], v11 offset:9216\n"
"v_mfma_f32_16x16x32_bf16 v[60:63], a[228:231], v[156:159], v[60:63]\n"
"ds_read_b128 a[224:227], v11 offset:10240\n"
"s_waitcnt lgkmcnt(7)\n"
"v_mfma_f32_16x16x32_bf16 v[64:67], a[232:235], v[144:147], v[64:67]\n"
"ds_read_b128 a[228:231], v10 offset:34816\n"
"s_waitcnt vmcnt(11)\n"
"v_mfma_f32_16x16x32_bf16 v[48:51], a[236:239], v[160:163], v[48:51]\n"
"ds_read_b128 a[232:235], v11 offset:14336\n"
"v_mfma_f32_16x16x32_bf16 v[52:55], a[240:243], v[160:163], v[52:55]\n"
"ds_read_b128 a[236:239], v11 offset:15360\n"
"s_waitcnt lgkmcnt(7)\n"
"v_mfma_f32_16x16x32_bf16 v[56:59], a[244:247], v[160:163], v[56:59]\n"
"ds_read_b128 a[240:243], v11 offset:16384\n"
"v_mfma_f32_16x16x32_bf16 v[60:63], a[248:251], v[160:163], v[60:63]\n"
"ds_read_b128 a[244:247], v11 offset:17408\n"
"v_mfma_f32_16x16x32_bf16 v[68:71], a[252:255], v[144:147], v[68:71]\n"
"ds_read_b128 a[248:251], v11 offset:21504\n"
"s_waitcnt vmcnt(10) lgkmcnt(7)\n"
"v_smfmac_f32_16x16x64_bf16 v[24:27], v[20:23], v[160:167], v44\n"
"v_mfma_f32_16x16x32_bf16 v[48:51], v[40:43], v[164:167], v[48:51]\n"
"ds_read_b128 a[252:255], v11 offset:22528\n"
"s_waitcnt vmcnt(8)\n"
"v_readfirstlane_b32 s57, v6\n"
"s_cmp_lg_u32 s57, 16\n"
"s_cbranch_scc1 L_p4slow\n"
"L_p4back:\n"
"v_mfma_f32_16x16x32_bf16 v[52:55], v[120:123], v[164:167], v[52:55]\n"
"ds_read_b128 v[40:43], v11 offset:23552\n"
"buffer_load_dwordx4 v[76:79], v2, s[40:43], s8 offen offset:0 sc1\n"
"v_mfma_f32_16x16x32_bf16 v[56:59], v[220:223], v[164:167], v[56:59]\n"
"ds_read_b128 v[120:123], v11 offset:24576\n"
"s_waitcnt lgkmcnt(7)\n"
"v_mfma_f32_16x16x32_bf16 v[60:63], a[224:227], v[164:167], v[60:63]\n"
"ds_read_b128 v[220:223], v11 offset:28672\n"
"buffer_load_dwordx4 v[80:83], v2, s[40:43], s8 offen offset:1024 sc1\n"
"v_mfma_f32_16x16x32_bf16 v[72:75], a[228:231], v[144:147], v[72:75]\n"
"ds_read_b128 a[224:227], v11 offset:29696\n"
"v_mfma_f32_16x16x32_bf16 v[48:51], a[232:235], v[168:171], v[48:51]\n"
"ds_read_b128 a[228:231], v11 offset:30720\n"
"s_waitcnt lgkmcnt(7)\n"
"v_mfma_f32_16x16x32_bf16 v[52:55], a[236:239], v[168:171], v[52:55]\n"
"ds_read_b128 a[232:235], v11 offset:31744\n"
"buffer_load_dwordx4 v[84:87], v2, s[40:43], s8 offen offset:2048 sc1\n"
"v_mfma_f32_16x16x32_bf16 v[56:59], a[240:243], v[168:171], v[56:59]\n"
"ds_read_b128 a[236:239], v11 offset:35840\n"
"v_mfma_f32_16x16x32_bf16 v[60:63], a[244:247], v[168:171], v[60:63]\n"
"ds_read_b128 a[240:243], v11 offset:36864\n"
"s_waitcnt vmcnt(10) lgkmcnt(7)\n"
"v_smfmac_f32_16x16x64_bf16 v[24:27], v[20:23], v[168:175], v44\n"
"v_mfma_f32_16x16x32_bf16 v[48:51], a[248:251], v[172:175], v[48:51]\n"
"ds_read_b128 a[244:247], v11 offset:37888\n"
"buffer_load_dwordx4 v[88:91], v2, s[40:43], s8 offen offset:3072 sc1\n"
"v_mfma_f32_16x16x32_bf16 v[52:55], a[252:255], v[172:175], v[52:55]\n"
"ds_read_b128 a[248:251], v11 offset:38912\n"
"v_mfma_f32_16x16x32_bf16 v[56:59], v[40:43], v[172:175], v[56:59]\n"
"ds_read_b128 a[252:255], v11 offset:43008\n"
"s_waitcnt lgkmcnt(7)\n"
"v_mfma_f32_16x16x32_bf16 v[60:63], v[120:123], v[172:175], v[60:63]\n"
"ds_read_b128 v[40:43], v11 offset:44032\n"
"s_add_u32 s8, s8, 4096\n"
"buffer_load_dwordx4 v[92:95], v2, s[40:43], s8 offen offset:0 sc1\n"
"s_waitcnt vmcnt(11)\n"
"v_mfma_f32_16x16x32_bf16 v[48:51], v[220:223], v[176:179], v[48:51]\n"
"ds_read_b128 v[120:123], v11 offset:45056\n"
"v_mfma_f32_16x16x32_bf16 v[52:55], a[224:227], v[176:179], v[52:55]\n"
"ds_read_b128 v[220:223], v11 offset:46080\n"
"s_waitcnt lgkmcnt(7)\n"
"v_mfma_f32_16x16x32_bf16 v[56:59], a[228:231], v[176:179], v[56:59]\n"
"ds_read_b128 a[224:227], v11 offset:50176\n"
"buffer_load_dwordx4 v[96:99], v2, s[40:43], s8 offen offset:1024 sc1\n"
"v_mfma_f32_16x16x32_bf16 v[60:63], a[232:235], v[176:179], v[60:63]\n"
"ds_read_b128 a[228:231], v11 offset:51200\n"
"s_waitcnt vmcnt(11)\n"
"v_smfmac_f32_16x16x64_bf16 v[24:27], v[20:23], v[176:183], v44\n"
"v_mfma_f32_16x16x32_bf16 v[48:51], a[236:239], v[180:183], v[48:51]\n"
"ds_read_b128 a[232:235], v11 offset:52224\n"
"s_waitcnt lgkmcnt(7)\n"
"v_mfma_f32_16x16x32_bf16 v[52:55], a[240:243], v[180:183], v[52:55]\n"
"ds_read_b128 a[236:239], v11 offset:53248\n"
"buffer_load_dwordx4 v[100:103], v2, s[40:43], s8 offen offset:2048 sc1\n"
"v_mfma_f32_16x16x32_bf16 v[56:59], a[244:247], v[180:183], v[56:59]\n"
"ds_read_b128 a[240:243], v10 offset:39936\n"
"v_mfma_f32_16x16x32_bf16 v[60:63], a[248:251], v[180:183], v[60:63]\n"
"ds_read_b128 a[244:247], v10 offset:40960\n"
"s_waitcnt vmcnt(11) lgkmcnt(7)\n"
"v_mfma_f32_16x16x32_bf16 v[48:51], a[252:255], v[184:187], v[48:51]\n"
"ds_read_b128 a[248:251], v10 offset:41984\n"
"buffer_load_dwordx4 v[104:107], v2, s[40:43], s8 offen offset:3072 sc1\n"
"v_mfma_f32_16x16x32_bf16 v[52:55], v[40:43], v[184:187], v[52:55]\n"
"ds_read_b128 a[252:255], v10 offset:47104\n"
"v_mfma_f32_16x16x32_bf16 v[56:59], v[120:123], v[184:187], v[56:59]\n"
"ds_read_b128 v[40:43], v10 offset:48128\n"
"s_waitcnt lgkmcnt(7)\n"
"v_mfma_f32_16x16x32_bf16 v[60:63], v[220:223], v[184:187], v[60:63]\n"
"ds_read_b128 v[120:123], v10 offset:49152\n"
"s_waitcnt vmcnt(8)\n"
"s_barrier\n"
"buffer_atomic_add v7, v3, s[40:43], 0 offen offset:392\n"
"v_smfmac_f32_16x16x64_bf16 v[24:27], v[20:23], v[184:191], v44\n"
"v_mfma_f32_16x16x32_bf16 v[48:51], a[224:227], v[188:191], v[48:51]\n"
"ds_read_b128 v[220:223], v10 offset:54272\n"
"s_add_u32 s8, s8, 4096\n"
"buffer_load_dwordx4 v[108:111], v2, s[40:43], s8 offen offset:0 sc1\n"
"v_mfma_f32_16x16x32_bf16 v[52:55], a[228:231], v[188:191], v[52:55]\n"
"ds_read_b128 a[224:227], v10 offset:55296\n"
"s_waitcnt lgkmcnt(7)\n"
"v_mfma_f32_16x16x32_bf16 v[56:59], a[232:235], v[188:191], v[56:59]\n"
"ds_read_b128 a[228:231], v10 offset:56320\n"
"buffer_load_dwordx4 v[192:195], v2, s[40:43], s8 offen offset:1024 sc1\n"
"v_add_f32 v24, v24, v25\n"
"v_mul_f32 v38, 0xbb000000, v24\n"
"v_mfma_f32_16x16x32_bf16 v[60:63], a[236:239], v[188:191], v[60:63]\n"
"ds_read_b128 a[232:235], v11 offset:4096\n"
"v_fmac_f32 v48, v38, v224\n"
"v_fmac_f32 v49, v38, v225\n"
"v_mfma_f32_16x16x32_bf16 v[64:67], a[240:243], v[148:151], v[64:67]\n"
"ds_read_b128 a[236:239], v11 offset:5120\n"
"v_fmac_f32 v50, v38, v226\n"
"v_fmac_f32 v51, v38, v227\n"
"v_fmac_f32 v52, v38, v228\n"
"s_waitcnt lgkmcnt(7)\n"
"v_mfma_f32_16x16x32_bf16 v[68:71], a[244:247], v[148:151], v[68:71]\n"
"ds_read_b128 a[240:243], v11 offset:6144\n"
"buffer_load_dwordx4 v[196:199], v2, s[40:43], s8 offen offset:2048 sc1\n"
"v_fmac_f32 v53, v38, v229\n"
"v_fmac_f32 v54, v38, v230\n"
"v_mfma_f32_16x16x32_bf16 v[72:75], a[248:251], v[148:151], v[72:75]\n"
"ds_read_b128 a[244:247], v11 offset:11264\n"
"v_fmac_f32 v55, v38, v231\n"
"v_cvt_pk_bf16_f32 v48, v48, v49\n"
"v_cvt_pk_bf16_f32 v49, v50, v51\n"
"v_mfma_f32_16x16x32_bf16 v[64:67], a[252:255], v[152:155], v[64:67]\n"
"ds_read_b128 a[248:251], v11 offset:12288\n"
"v_cvt_pk_bf16_f32 v50, v52, v53\n"
"v_cvt_pk_bf16_f32 v51, v54, v55\n"
"s_waitcnt lgkmcnt(7)\n"
"v_mfma_f32_16x16x32_bf16 v[68:71], v[40:43], v[152:155], v[68:71]\n"
"ds_read_b128 a[252:255], v11 offset:13312\n"
"buffer_load_dwordx4 v[200:203], v2, s[40:43], s8 offen offset:3072 sc1\n"
"v_pk_max_i16 v50, v50, 0\n"
"v_pk_max_i16 v48, v48, 0\n"
"v_mfma_f32_16x16x32_bf16 v[72:75], v[120:123], v[152:155], v[72:75]\n"
"ds_read_b128 v[40:43], v11 offset:18432\n"
"v_pk_max_i16 v51, v51, 0\n"
"v_pk_max_i16 v49, v49, 0\n"
"v_mfma_f32_16x16x32_bf16 v[64:67], v[220:223], v[156:159], v[64:67]\n"
"ds_read_b128 v[120:123], v11 offset:19456\n"
"s_add_u32 s72, s72, 90112\n"
"v_permlane16_swap_b32 v48, v50\n"
"v_permlane16_swap_b32 v49, v51\n"
"s_waitcnt lgkmcnt(7)\n"
"v_mfma_f32_16x16x32_bf16 v[68:71], a[224:227], v[156:159], v[68:71]\n"
"ds_read_b128 v[220:223], v11 offset:20480\n"
"buffer_store_dwordx4 v[48:51], v45, s[40:43], s72 offen sc1\n"
"v_fmac_f32 v56, v38, v232\n"
"v_mfma_f32_16x16x32_bf16 v[72:75], a[228:231], v[156:159], v[72:75]\n"
"ds_read_b128 a[224:227], v11 offset:25600\n"
"v_fmac_f32 v57, v38, v233\n"
"v_fmac_f32 v58, v38, v234\n"
"v_fmac_f32 v59, v38, v235\n"
"v_mfma_f32_16x16x32_bf16 v[64:67], a[232:235], v[160:163], v[64:67]\n"
"ds_read_b128 a[228:231], v11 offset:26624\n"
"v_fmac_f32 v60, v38, v236\n"
"v_fmac_f32 v61, v38, v237\n"
"s_waitcnt lgkmcnt(7)\n"
"v_mfma_f32_16x16x32_bf16 v[68:71], a[236:239], v[160:163], v[68:71]\n"
"ds_read_b128 a[232:235], v11 offset:27648\n"
"v_fmac_f32 v62, v38, v238\n"
"v_fmac_f32 v63, v38, v239\n"
"v_cvt_pk_bf16_f32 v56, v56, v57\n"
"v_mfma_f32_16x16x32_bf16 v[72:75], a[240:243], v[160:163], v[72:75]\n"
"ds_read_b128 a[236:239], v11 offset:32768\n"
"v_cvt_pk_bf16_f32 v57, v58, v59\n"
"v_cvt_pk_bf16_f32 v58, v60, v61\n"
"v_mfma_f32_16x16x32_bf16 v[64:67], a[244:247], v[164:167], v[64:67]\n"
"ds_read_b128 a[240:243], v11 offset:33792\n"
"v_cvt_pk_bf16_f32 v59, v62, v63\n"
"v_pk_max_i16 v58, v58, 0\n"
"v_pk_max_i16 v56, v56, 0\n"
"s_waitcnt lgkmcnt(7)\n"
"v_mfma_f32_16x16x32_bf16 v[68:71], a[248:251], v[164:167], v[68:71]\n"
"ds_read_b128 a[244:247], v11 offset:34816\n"
"v_pk_max_i16 v59, v59, 0\n"
"v_pk_max_i16 v57, v57, 0\n"
"v_mfma_f32_16x16x32_bf16 v[72:75], a[252:255], v[164:167], v[72:75]\n"
"ds_read_b128 a[248:251], v11 offset:39936\n"
"s_add_u32 s72, s72, 8192\n"
"v_permlane16_swap_b32 v56, v58\n"
"v_permlane16_swap_b32 v57, v59\n"
"v_mfma_f32_16x16x32_bf16 v[64:67], v[40:43], v[168:171], v[64:67]\n"
"ds_read_b128 a[252:255], v11 offset:40960\n"
"buffer_store_dwordx4 v[56:59], v45, s[40:43], s72 offen sc1\n"
"s_waitcnt lgkmcnt(7)\n"
"v_mfma_f32_16x16x32_bf16 v[68:71], v[120:123], v[168:171], v[68:71]\n"
"ds_read_b128 v[40:43], v11 offset:41984\n"
"v_mfma_f32_16x16x32_bf16 v[72:75], v[220:223], v[168:171], v[72:75]\n"
"ds_read_b128 v[120:123], v11 offset:47104\n"
"v_mfma_f32_16x16x32_bf16 v[64:67], a[224:227], v[172:175], v[64:67]\n"
"ds_read_b128 v[220:223], v11 offset:48128\n"
"s_waitcnt lgkmcnt(7)\n"
"v_mfma_f32_16x16x32_bf16 v[68:71], a[228:231], v[172:175], v[68:71]\n"
"ds_read_b128 a[224:227], v11 offset:49152\n"
"v_mfma_f32_16x16x32_bf16 v[72:75], a[232:235], v[172:175], v[72:75]\n"
"ds_read_b128 a[228:231], v11 offset:54272\n"
"v_mfma_f32_16x16x32_bf16 v[64:67], a[236:239], v[176:179], v[64:67]\n"
"ds_read_b128 a[232:235], v11 offset:55296\n"
"s_waitcnt lgkmcnt(7)\n"
"v_mfma_f32_16x16x32_bf16 v[68:71], a[240:243], v[176:179], v[68:71]\n"
"ds_read_b128 a[236:239], v11 offset:56320\n"
"v_mfma_f32_16x16x32_bf16 v[72:75], a[244:247], v[176:179], v[72:75]\n"
"v_mfma_f32_16x16x32_bf16 v[64:67], a[248:251], v[180:183], v[64:67]\n"
"s_waitcnt lgkmcnt(5)\n"
"v_mfma_f32_16x16x32_bf16 v[68:71], a[252:255], v[180:183], v[68:71]\n"
"v_mfma_f32_16x16x32_bf16 v[72:75], v[40:43], v[180:183], v[72:75]\n"
"v_mfma_f32_16x16x32_bf16 v[64:67], v[120:123], v[184:187], v[64:67]\n"
"v_mov_b32 v8, 0\n"
"buffer_atomic_add v8, v4, s[40:43], s61 offen offset:392 sc0\n"
"s_waitcnt lgkmcnt(2)\n"
"v_mfma_f32_16x16x32_bf16 v[68:71], v[220:223], v[184:187], v[68:71]\n"
"v_mfma_f32_16x16x32_bf16 v[72:75], a[224:227], v[184:187], v[72:75]\n"
"v_mfma_f32_16x16x32_bf16 v[64:67], a[228:231], v[188:191], v[64:67]\n"
"s_waitcnt lgkmcnt(0)\n"
"v_mfma_f32_16x16x32_bf16 v[68:71], a[232:235], v[188:191], v[68:71]\n"
"v_mov_b32 v9, 0\n"
"buffer_atomic_add v9, v4, s[40:43], s61 offen offset:392 sc0\n"
"v_mfma_f32_16x16x32_bf16 v[72:75], a[236:239], v[188:191], v[72:75]\n"
"v_mfma_f32_16x16x32_bf16 v[32:35], v[144:147], v[144:147], v[32:35]\n"
"v_mfma_f32_16x16x32_bf16 v[32:35], v[148:151], v[148:151], v[32:35]\n"
"s_nop 0\n"
"v_fmac_f32 v64, v38, v240\n"
"v_fmac_f32 v65, v38, v241\n"
"v_fmac_f32 v66, v38, v242\n"
"v_mfma_f32_16x16x32_bf16 v[32:35], v[152:155], v[152:155], v[32:35]\n"
"v_fmac_f32 v67, v38, v243\n"
"v_fmac_f32 v68, v38, v244\n"
"v_fmac_f32 v69, v38, v245\n"
"v_mfma_f32_16x16x32_bf16 v[32:35], v[156:159], v[156:159], v[32:35]\n"
"v_fmac_f32 v70, v38, v246\n"
"v_fmac_f32 v71, v38, v247\n"
"v_cvt_pk_bf16_f32 v64, v64, v65\n"
"v_mfma_f32_16x16x32_bf16 v[32:35], v[160:163], v[160:163], v[32:35]\n"
"v_cvt_pk_bf16_f32 v65, v66, v67\n"
"v_cvt_pk_bf16_f32 v66, v68, v69\n"
"v_cvt_pk_bf16_f32 v67, v70, v71\n"
"v_mfma_f32_16x16x32_bf16 v[32:35], v[164:167], v[164:167], v[32:35]\n"
"v_pk_max_i16 v66, v66, 0\n"
"v_pk_max_i16 v64, v64, 0\n"
"v_pk_max_i16 v67, v67, 0\n"
"v_mfma_f32_16x16x32_bf16 v[32:35], v[168:171], v[168:171], v[32:35]\n"
"v_pk_max_i16 v65, v65, 0\n"
"s_add_u32 s72, s72, 8192\n"
"v_permlane16_swap_b32 v64, v66\n"
"v_mfma_f32_16x16x32_bf16 v[32:35], v[172:175], v[172:175], v[32:35]\n"
"v_permlane16_swap_b32 v65, v67\n"
"buffer_store_dwordx4 v[64:67], v45, s[40:43], s72 offen sc1\n"
"v_fmac_f32 v72, v38, v248\n"
"v_mfma_f32_16x16x32_bf16 v[32:35], v[176:179], v[176:179], v[32:35]\n"
"v_fmac_f32 v73, v38, v249\n"
"v_fmac_f32 v74, v38, v250\n"
"v_fmac_f32 v75, v38, v251\n"
"v_mfma_f32_16x16x32_bf16 v[32:35], v[180:183], v[180:183], v[32:35]\n"
"v_cvt_pk_bf16_f32 v72, v72, v73\n"
"v_cvt_pk_bf16_f32 v73, v74, v75\n"
"v_pk_max_i16 v74, v72, 0\n"
"v_mfma_f32_16x16x32_bf16 v[32:35], v[184:187], v[184:187], v[32:35]\n"
"v_pk_max_i16 v72, v72, 0\n"
"v_pk_max_i16 v75, v73, 0\n"
"v_pk_max_i16 v73, v73, 0\n"
"v_mfma_f32_16x16x32_bf16 v[32:35], v[188:191], v[188:191], v[32:35]\n"
"s_add_u32 s72, s72, 8192\n"
"v_permlane16_swap_b32 v72, v74\n"
"v_permlane16_swap_b32 v73, v75\n"
"buffer_store_dwordx4 v[72:75], v13, s[40:43], s72 offen sc1\n"
"s_waitcnt vmcnt(18)\n"
"v_mfma_f32_32x32x16_bf16 v[144:159], a[4:7], v[76:79], 0\n"
"s_add_u32 s8, s8, 4096\n"
"buffer_load_dwordx4 v[204:207], v2, s[40:43], s8 offen offset:0 sc1\n"
"v_cndmask_b32 v32, v32, v33, s[66:67]\n"
"v_cndmask_b32 v34, v34, v35, s[66:67]\n"
"v_mfma_f32_32x32x16_bf16 v[128:143], a[0:3], v[76:79], 0\n"
"v_mul_f32 v33, v38, v38\n"
"v_cndmask_b32 v32, v32, v34, s[76:77]\n"
"s_waitcnt vmcnt(18)\n"
"v_mfma_f32_32x32x16_bf16 v[144:159], a[60:63], v[80:83], v[144:159]\n"
"buffer_load_dwordx4 v[208:211], v2, s[40:43], s8 offen offset:1024 sc1\n"
"v_fma_f32 v32, v32, s9, -v33\n"
"v_max_f32 v32, 0, v32\n"
"v_mfma_f32_32x32x16_bf16 v[128:143], a[56:59], v[80:83], v[128:143]\n"
"v_add_f32 v32, 0x3727c5ac, v32\n"
"v_rsq_f32 v32, v32\n"
"s_waitcnt vmcnt(18)\n"
"v_mfma_f32_32x32x16_bf16 v[144:159], a[116:119], v[84:87], v[144:159]\n"
"buffer_load_dwordx4 v[212:215], v2, s[40:43], s8 offen offset:2048 sc1\n"
"ds_write_b32 v46, v32 offset:42880\n"
"v_mfma_f32_32x32x16_bf16 v[128:143], a[112:115], v[84:87], v[128:143]\n"
"s_waitcnt vmcnt(18)\n"
"v_mfma_f32_32x32x16_bf16 v[144:159], a[172:175], v[88:91], v[144:159]\n"
"buffer_load_dwordx4 v[216:219], v2, s[40:43], s8 offen offset:3072 sc1\n"
"v_mfma_f32_32x32x16_bf16 v[128:143], a[168:171], v[88:91], v[128:143]\n"
"s_waitcnt lgkmcnt(0)\n"
"s_waitcnt vmcnt(8)\n"
"s_barrier\n"
"buffer_atomic_add v7, v3, s[40:43], 0 offen offset:268\n"
"v_mfma_f32_32x32x16_bf16 v[144:159], a[12:15], v[92:95], v[144:159]\n"
"ds_read_b32 v40, v47 offset:42752\n"
"ds_read_b32 v42, v47 offset:42880\n"
"v_mfma_f32_32x32x16_bf16 v[128:143], a[8:11], v[92:95], v[128:143]\n"
"v_lshlrev_b32 v20, 16, v112\n"
"v_and_b32 v21, 0xffff0000, v112\n"
"v_mfma_f32_32x32x16_bf16 v[144:159], a[68:71], v[96:99], v[144:159]\n"
"s_waitcnt vmcnt(8)\n"
"v_readfirstlane_b32 s57, v8\n"
"s_cmp_lg_u32 s57, s4\n"
"s_cbranch_scc1 L_p5slow\n"
"L_p5back:\n"
"s_barrier\n"
"s_add_u32 s8, s8, 4096\n"
"buffer_load_dwordx4 v[76:79], v2, s[40:43], s8 offen offset:0 sc1\n"
"v_lshlrev_b32 v22, 16, v113\n"
"v_and_b32 v23, 0xffff0000, v113\n"
"v_mfma_f32_32x32x16_bf16 v[128:143], a[64:67], v[96:99], v[128:143]\n"
"v_lshlrev_b32 v24, 16, v114\n"
"v_and_b32 v25, 0xffff0000, v114\n"
"v_mfma_f32_32x32x16_bf16 v[144:159], a[124:127], v[100:103], v[144:159]\n"
"buffer_load_dwordx4 v[80:83], v2, s[40:43], s8 offen offset:1024 sc1\n"
"v_lshlrev_b32 v26, 16, v115\n"
"v_and_b32 v27, 0xffff0000, v115\n"
"v_mfma_f32_32x32x16_bf16 v[128:143], a[120:123], v[100:103], v[128:143]\n"
"v_lshlrev_b32 v28, 16, v116\n"
"v_and_b32 v29, 0xffff0000, v116\n"
"v_mfma_f32_32x32x16_bf16 v[144:159], a[180:183], v[104:107], v[144:159]\n"
"buffer_load_dwordx4 v[84:87], v2, s[40:43], s8 offen offset:2048 sc1\n"
"v_lshlrev_b32 v30, 16, v117\n"
"v_and_b32 v31, 0xffff0000, v117\n"
"v_mfma_f32_32x32x16_bf16 v[128:143], a[176:179], v[104:107], v[128:143]\n"
"v_lshlrev_b32 v32, 16, v118\n"
"v_and_b32 v33, 0xffff0000, v118\n"
"v_mfma_f32_32x32x16_bf16 v[144:159], a[20:23], v[108:111], v[144:159]\n"
"buffer_load_dwordx4 v[88:91], v2, s[40:43], s8 offen offset:3072 sc1\n"
"v_lshlrev_b32 v34, 16, v119\n"
"v_and_b32 v35, 0xffff0000, v119\n"
"v_mfma_f32_32x32x16_bf16 v[128:143], a[16:19], v[108:111], v[128:143]\n"
"v_mov_b32 v6, 0\n"
"buffer_atomic_add v6, v5, s[40:43], s61 offen offset:268 sc0\n"
"v_mfma_f32_32x32x16_bf16 v[144:159], a[76:79], v[192:195], v[144:159]\n"
"s_add_u32 s8, s8, 4096\n"
"buffer_load_dwordx4 v[92:95], v2, s[40:43], s8 offen offset:0 sc1\n"
"v_mfma_f32_32x32x16_bf16 v[128:143], a[72:75], v[192:195], v[128:143]\n"
"s_waitcnt vmcnt(11)\n"
"s_barrier\n"
"buffer_atomic_add v7, v3, s[40:43], 0 offen offset:396\n"
"v_mfma_f32_32x32x16_bf16 v[144:159], a[132:135], v[196:199], v[144:159]\n"
"buffer_load_dwordx4 v[96:99], v2, s[40:43], s8 offen offset:1024 sc1\n"
"v_mfma_f32_32x32x16_bf16 v[128:143], a[128:131], v[196:199], v[128:143]\n"
"v_mfma_f32_32x32x16_bf16 v[144:159], a[188:191], v[200:203], v[144:159]\n"
"buffer_load_dwordx4 v[100:103], v2, s[40:43], s8 offen offset:2048 sc1\n"
"v_mfma_f32_32x32x16_bf16 v[128:143], a[184:187], v[200:203], v[128:143]\n"
"s_waitcnt vmcnt(13)\n"
"v_mfma_f32_32x32x16_bf16 v[144:159], a[28:31], v[204:207], v[144:159]\n"
"buffer_load_dwordx4 v[104:107], v2, s[40:43], s8 offen offset:3072 sc1\n"
"v_mfma_f32_32x32x16_bf16 v[128:143], a[24:27], v[204:207], v[128:143]\n"
"v_mov_b32 v18, 0\n"
"buffer_atomic_add v18, v5, s[40:43], s61 offen offset:268 sc0\n"
"L_pada:\n"
"s_cbranch_execz L_ret0\n"
"s_waitcnt vmcnt(14)\n"
"v_mfma_f32_32x32x16_bf16 v[144:159], a[84:87], v[208:211], v[144:159]\n"
"s_add_u32 s8, s8, 4096\n"
"buffer_load_dwordx4 v[108:111], v2, s[40:43], s8 offen offset:0 sc1\n"
"v_mfma_f32_32x32x16_bf16 v[128:143], a[80:83], v[208:211], v[128:143]\n"
"s_waitcnt vmcnt(14)\n"
"v_mfma_f32_32x32x16_bf16 v[144:159], a[140:143], v[212:215], v[144:159]\n"
"buffer_load_dwordx4 v[192:195], v2, s[40:43], s8 offen offset:1024 sc1\n"
"v_mfma_f32_32x32x16_bf16 v[128:143], a[136:139], v[212:215], v[128:143]\n"
"s_waitcnt vmcnt(14)\n"
"v_mfma_f32_32x32x16_bf16 v[144:159], a[196:199], v[216:219], v[144:159]\n"
"buffer_load_dwordx4 v[196:199], v2, s[40:43], s8 offen offset:2048 sc1\n"
"v_mfma_f32_32x32x16_bf16 v[128:143], a[192:195], v[216:219], v[128:143]\n"
"s_waitcnt vmcnt(13)\n"
"v_mfma_f32_32x32x16_bf16 v[144:159], a[36:39], v[76:79], v[144:159]\n"
"buffer_load_dwordx4 v[200:203], v2, s[40:43], s8 offen offset:3072 sc1\n"
"v_mfma_f32_32x32x16_bf16 v[128:143], a[32:35], v[76:79], v[128:143]\n"
"s_waitcnt vmcnt(13)\n"
"v_mfma_f32_32x32x16_bf16 v[144:159], a[92:95], v[80:83], v[144:159]\n"
"s_waitcnt vmcnt(10)\n"
"v_readfirstlane_b32 s57, v6\n"
"s_cmp_lg_u32 s57, 16\n"
"s_cbranch_scc1 L_p6slow\n"
"L_p6back:\n"
"s_add_u32 s8, s8, 90112\n"
"buffer_load_dwordx4 v[204:207], v2, s[40:43], s8 offen offset:0 sc1\n"
"v_mfma_f32_32x32x16_bf16 v[128:143], a[88:91], v[80:83], v[128:143]\n"
"v_mfma_f32_32x32x16_bf16 v[144:159], a[148:151], v[84:87], v[144:159]\n"
"buffer_load_dwordx4 v[208:211], v2, s[40:43], s8 offen offset:1024 sc1\n"
"v_mfma_f32_32x32x16_bf16 v[128:143], a[144:147], v[84:87], v[128:143]\n"
"v_mov_b32 v8, 0\n"
"buffer_atomic_add v8, v5, s[40:43], s61 offen offset:396 sc0\n"
"v_mfma_f32_32x32x16_bf16 v[144:159], a[204:207], v[88:91], v[144:159]\n"
"buffer_load_dwordx4 v[212:215], v2, s[40:43], s8 offen offset:2048 sc1\n"
"v_mfma_f32_32x32x16_bf16 v[128:143], a[200:203], v[88:91], v[128:143]\n"
"s_waitcnt vmcnt(13)\n"
"v_mfma_f32_32x32x16_bf16 v[144:159], a[44:47], v[92:95], v[144:159]\n"
"buffer_load_dwordx4 v[216:219], v2, s[40:43], s8 offen offset:3072 sc1\n"
"v_mfma_f32_32x32x16_bf16 v[128:143], a[40:43], v[92:95], v[128:143]\n"
"s_waitcnt vmcnt(12)\n"
"v_mfma_f32_32x32x16_bf16 v[144:159], a[100:103], v[96:99], v[144:159]\n"
"s_add_u32 s8, s8, 4096\n"
"buffer_load_dwordx4 v[76:79], v2, s[40:43], s8 offen offset:0 sc1\n"
"v_mfma_f32_32x32x16_bf16 v[128:143], a[96:99], v[96:99], v[128:143]\n"
"s_waitcnt vmcnt(12)\n"
"v_mfma_f32_32x32x16_bf16 v[144:159], a[156:159], v[100:103], v[144:159]\n"
"buffer_load_dwordx4 v[80:83], v2, s[40:43], s8 offen offset:1024 sc1\n"
"v_mfma_f32_32x32x16_bf16 v[128:143], a[152:155], v[100:103], v[128:143]\n"
"v_mov_b32 v9, 0\n"
"buffer_atomic_add v9, v5, s[40:43], s61 offen offset:396 sc0\n"
"s_waitcnt vmcnt(13)\n"
"v_mfma_f32_32x32x16_bf16 v[144:159], a[212:215], v[104:107], v[144:159]\n"
"buffer_load_dwordx4 v[84:87], v2, s[40:43], s8 offen offset:2048 sc1\n"
"v_mfma_f32_32x32x16_bf16 v[128:143], a[208:211], v[104:107], v[128:143]\n"
"s_waitcnt vmcnt(12)\n"
"v_mfma_f32_32x32x16_bf16 v[144:159], a[52:55], v[108:111], v[144:159]\n"
"buffer_load_dwordx4 v[88:91], v2, s[40:43], s8 offen offset:3072 sc1\n"
"v_mfma_f32_32x32x16_bf16 v[128:143], a[48:51], v[108:111], v[128:143]\n"
"s_waitcnt vmcnt(12)\n"
"v_mfma_f32_32x32x16_bf16 v[144:159], a[108:111], v[192:195], v[144:159]\n"
"s_add_u32 s8, s8, 4096\n"
"buffer_load_dwordx4 v[92:95], v2, s[40:43], s8 offen offset:0 sc1\n"
"v_mfma_f32_32x32x16_bf16 v[128:143], a[104:107], v[192:195], v[128:143]\n"
"s_waitcnt vmcnt(12)\n"
"v_mfma_f32_32x32x16_bf16 v[144:159], a[164:167], v[196:199], v[144:159]\n"
"buffer_load_dwordx4 v[96:99], v2, s[40:43], s8 offen offset:1024 sc1\n"
"v_mfma_f32_32x32x16_bf16 v[128:143], a[160:163], v[196:199], v[128:143]\n"
"s_waitcnt vmcnt(12)\n"
"v_mfma_f32_32x32x16_bf16 v[144:159], a[220:223], v[200:203], v[144:159]\n"
"buffer_load_dwordx4 v[100:103], v2, s[40:43], s8 offen offset:2048 sc1\n"
"v_mfma_f32_32x32x16_bf16 v[128:143], a[216:219], v[200:203], v[128:143]\n"
"s_waitcnt vmcnt(12)\n"
"v_mfma_f32_32x32x16_bf16 v[176:191], a[4:7], v[204:207], 0\n"
"buffer_load_dwordx4 v[104:107], v2, s[40:43], s8 offen offset:3072 sc1\n"
"v_mfma_f32_32x32x16_bf16 v[160:175], a[0:3], v[204:207], 0\n"
"s_waitcnt vmcnt(12)\n"
"v_mfma_f32_32x32x16_bf16 v[176:191], a[60:63], v[208:211], v[176:191]\n"
"s_add_u32 s8, s8, 4096\n"
"buffer_load_dwordx4 v[108:111], v2, s[40:43], s8 offen offset:0 sc1\n"
"v_mfma_f32_32x32x16_bf16 v[160:175], a[56:59], v[208:211], v[160:175]\n"
"s_waitcnt vmcnt(11)\n"
"v_mfma_f32_32x32x16_bf16 v[176:191], a[116:119], v[212:215], v[176:191]\n"
"buffer_load_dwordx4 v[192:195], v2, s[40:43], s8 offen offset:1024 sc1\n"
"v_mfma_f32_32x32x16_bf16 v[160:175], a[112:115], v[212:215], v[160:175]\n"
"s_waitcnt vmcnt(11)\n"
"v_mfma_f32_32x32x16_bf16 v[176:191], a[172:175], v[216:219], v[176:191]\n"
"buffer_load_dwordx4 v[196:199], v2, s[40:43], s8 offen offset:2048 sc1\n"
"ds_write_b128 v127, v[144:147] offset:4096\n"
"v_mfma_f32_32x32x16_bf16 v[160:175], a[168:171], v[216:219], v[160:175]\n"
"ds_write_b128 v127, v[148:151] offset:5120\n"
"s_waitcnt vmcnt(11)\n"
"v_mfma_f32_32x32x16_bf16 v[176:191], a[12:15], v[76:79], v[176:191]\n"
"buffer_load_dwordx4 v[200:203], v2, s[40:43], s8 offen offset:3072 sc1\n"
"ds_write_b128 v12, v[152:155] offset:6144\n"
"v_mfma_f32_32x32x16_bf16 v[160:175], a[8:11], v[76:79], v[160:175]\n"
"ds_write_b128 v12, v[156:159] offset:7168\n"
"s_waitcnt vmcnt(11)\n"
"v_mfma_f32_32x32x16_bf16 v[176:191], a[68:71], v[80:83], v[176:191]\n"
"v_readfirstlane_b32 s57, v8\n"
"s_cmp_lg_u32 s57, 16\n"
"s_cbranch_scc1 L_p7slow\n"
"L_p7back:\n"
"s_add_u32 s8, s8, 4096\n"
"buffer_load_dwordx4 v[204:207], v2, s[40:43], s8 offen offset:0 sc1\n"
"ds_write_b128 v126, v[136:139] offset:2048\n"
"v_mfma_f32_32x32x16_bf16 v[160:175], a[64:67], v[80:83], v[160:175]\n"
"ds_write_b128 v126, v[140:143] offset:3072\n"
"s_waitcnt vmcnt(10)\n"
"v_mfma_f32_32x32x16_bf16 v[176:191], a[124:127], v[84:87], v[176:191]\n"
"buffer_load_dwordx4 v[208:211], v2, s[40:43], s8 offen offset:1024 sc1\n"
"v_mfma_f32_32x32x16_bf16 v[160:175], a[120:123], v[84:87], v[160:175]\n"
"s_waitcnt vmcnt(10)\n"
"s_waitcnt lgkmcnt(0)\n"
"s_barrier\n"
"v_mfma_f32_32x32x16_bf16 v[176:191], a[180:183], v[88:91], v[176:191]\n"
"buffer_load_dwordx4 v[212:215], v2, s[40:43], s8 offen offset:2048 sc1\n"
"ds_read_b128 v[224:227], v125 offset:2048\n"
"ds_read_b128 v[232:235], v125 offset:4096\n"
"v_mfma_f32_32x32x16_bf16 v[160:175], a[176:179], v[88:91], v[160:175]\n"
"ds_read_b128 v[240:243], v125 offset:6144\n"
"s_waitcnt vmcnt(10)\n"
"v_mfma_f32_32x32x16_bf16 v[176:191], a[20:23], v[92:95], v[176:191]\n"
"buffer_load_dwordx4 v[216:219], v2, s[40:43], s8 offen offset:3072 sc1\n"
"ds_read_b128 v[228:231], v125 offset:3072\n"
"ds_read_b128 v[236:239], v125 offset:5120\n"
"v_mfma_f32_32x32x16_bf16 v[160:175], a[16:19], v[92:95], v[160:175]\n"
"ds_read_b128 v[244:247], v125 offset:7168\n"
"s_waitcnt vmcnt(10)\n"
"v_mfma_f32_32x32x16_bf16 v[176:191], a[76:79], v[96:99], v[176:191]\n"
"s_add_u32 s8, s8, 4096\n"
"buffer_load_dwordx4 v[76:79], v2, s[40:43], s8 offen offset:0 sc1\n"
"v_mfma_f32_32x32x16_bf16 v[160:175], a[72:75], v[96:99], v[160:175]\n"
"s_waitcnt vmcnt(10)\n"
"s_waitcnt lgkmcnt(0)\n"
"v_mfma_f32_32x32x16_bf16 v[176:191], a[132:135], v[100:103], v[176:191]\n"
"buffer_load_dwordx4 v[80:83], v2, s[40:43], s8 offen offset:1024 sc1\n"
"v_add_f32 v128, v128, v224\n"
"v_add_f32 v129, v129, v225\n"
"v_add_f32 v130, v130, v226\n"
"v_add_f32 v131, v131, v227\n"
"v_mfma_f32_32x32x16_bf16 v[160:175], a[128:131], v[100:103], v[160:175]\n"
"v_add_f32 v232, v232, v240\n"
"v_add_f32 v233, v233, v241\n"
"v_add_f32 v234, v234, v242\n"
"v_add_f32 v235, v235, v243\n"
"s_waitcnt vmcnt(10)\n"
"v_mfma_f32_32x32x16_bf16 v[176:191], a[188:191], v[104:107], v[176:191]\n"
"buffer_load_dwordx4 v[84:87], v2, s[40:43], s8 offen offset:2048 sc1\n"
"v_add_f32 v128, v128, v232\n"
"v_add_f32 v129, v129, v233\n"
"v_add_f32 v130, v130, v234\n"
"v_add_f32 v131, v131, v235\n"
"v_mfma_f32_32x32x16_bf16 v[160:175], a[184:187], v[104:107], v[160:175]\n"
"v_fma_f32 v128, v128, v40, v20\n"
"v_fma_f32 v129, v129, v40, v21\n"
"v_fma_f32 v130, v130, v40, v22\n"
"v_fma_f32 v131, v131, v40, v23\n"
"s_waitcnt vmcnt(10)\n"
"v_mfma_f32_32x32x16_bf16 v[176:191], a[28:31], v[108:111], v[176:191]\n"
"buffer_load_dwordx4 v[88:91], v2, s[40:43], s8 offen offset:3072 sc1\n"
"v_cvt_pk_bf16_f32 v128, v128, v129\n"
"v_cvt_pk_bf16_f32 v129, v130, v131\n"
"v_add_f32 v132, v132, v228\n"
"v_add_f32 v133, v133, v229\n"
"v_mfma_f32_32x32x16_bf16 v[160:175], a[24:27], v[108:111], v[160:175]\n"
"v_add_f32 v134, v134, v230\n"
"v_add_f32 v135, v135, v231\n"
"v_add_f32 v236, v236, v244\n"
"v_add_f32 v237, v237, v245\n"
"s_waitcnt vmcnt(10)\n"
"v_mfma_f32_32x32x16_bf16 v[176:191], a[84:87], v[192:195], v[176:191]\n"
"s_add_u32 s8, s8, 4096\n"
"buffer_load_dwordx4 v[92:95], v2, s[40:43], s8 offen offset:0 sc1\n"
"v_add_f32 v238, v238, v246\n"
"v_add_f32 v239, v239, v247\n"
"v_add_f32 v132, v132, v236\n"
"v_add_f32 v133, v133, v237\n"
"v_mfma_f32_32x32x16_bf16 v[160:175], a[80:83], v[192:195], v[160:175]\n"
"v_add_f32 v134, v134, v238\n"
"v_add_f32 v135, v135, v239\n"
"v_fma_f32 v132, v132, v40, v24\n"
"v_fma_f32 v133, v133, v40, v25\n"
"s_waitcnt vmcnt(10)\n"
"v_mfma_f32_32x32x16_bf16 v[176:191], a[140:143], v[196:199], v[176:191]\n"
"buffer_load_dwordx4 v[96:99], v2, s[40:43], s8 offen offset:1024 sc1\n"
"v_fma_f32 v134, v134, v40, v26\n"
"v_fma_f32 v135, v135, v40, v27\n"
"v_cvt_pk_bf16_f32 v130, v132, v133\n"
"v_cvt_pk_bf16_f32 v131, v134, v135\n"
"v_mfma_f32_32x32x16_bf16 v[160:175], a[136:139], v[196:199], v[160:175]\n"
"s_waitcnt vmcnt(10)\n"
"v_mfma_f32_32x32x16_bf16 v[176:191], a[196:199], v[200:203], v[176:191]\n"
"buffer_load_dwordx4 v[100:103], v2, s[40:43], s8 offen offset:2048 sc1\n"
"v_mfma_f32_32x32x16_bf16 v[160:175], a[192:195], v[200:203], v[160:175]\n"
"s_waitcnt vmcnt(10)\n"
"v_mfma_f32_32x32x16_bf16 v[176:191], a[36:39], v[204:207], v[176:191]\n"
"buffer_load_dwordx4 v[104:107], v2, s[40:43], s8 offen offset:3072 sc1\n"
"v_mfma_f32_32x32x16_bf16 v[160:175], a[32:35], v[204:207], v[160:175]\n"
"s_waitcnt vmcnt(10)\n"
"v_mfma_f32_32x32x16_bf16 v[176:191], a[92:95], v[208:211], v[176:191]\n"
"v_mfma_f32_32x32x16_bf16 v[160:175], a[88:91], v[208:211], v[160:175]\n"
"s_waitcnt vmcnt(9)\n"
"v_mfma_f32_32x32x16_bf16 v[176:191], a[148:151], v[212:215], v[176:191]\n"
"v_mfma_f32_32x32x16_bf16 v[160:175], a[144:147], v[212:215], v[160:175]\n"
"s_waitcnt vmcnt(8)\n"
"v_mfma_f32_32x32x16_bf16 v[176:191], a[204:207], v[216:219], v[176:191]\n"
"v_mfma_f32_32x32x16_bf16 v[160:175], a[200:203], v[216:219], v[160:175]\n"
"s_waitcnt vmcnt(7)\n"
"v_mfma_f32_32x32x16_bf16 v[176:191], a[44:47], v[76:79], v[176:191]\n"
"v_mfma_f32_32x32x16_bf16 v[160:175], a[40:43], v[76:79], v[160:175]\n"
"s_waitcnt vmcnt(6)\n"
"v_mfma_f32_32x32x16_bf16 v[176:191], a[100:103], v[80:83], v[176:191]\n"
"v_mfma_f32_32x32x16_bf16 v[160:175], a[96:99], v[80:83], v[160:175]\n"
"s_waitcnt vmcnt(5)\n"
"v_mfma_f32_32x32x16_bf16 v[176:191], a[156:159], v[84:87], v[176:191]\n"
"v_mfma_f32_32x32x16_bf16 v[160:175], a[152:155], v[84:87], v[160:175]\n"
"s_waitcnt vmcnt(4)\n"
"v_mfma_f32_32x32x16_bf16 v[176:191], a[212:215], v[88:91], v[176:191]\n"
"v_mfma_f32_32x32x16_bf16 v[160:175], a[208:211], v[88:91], v[160:175]\n"
"s_waitcnt vmcnt(3)\n"
"v_mfma_f32_32x32x16_bf16 v[176:191], a[52:55], v[92:95], v[176:191]\n"
"buffer_store_dwordx4 v[128:131], v124, s[48:51], s34 offen\n"
"v_mfma_f32_32x32x16_bf16 v[160:175], a[48:51], v[92:95], v[160:175]\n"
"s_waitcnt vmcnt(3)\n"
"v_mfma_f32_32x32x16_bf16 v[176:191], a[108:111], v[96:99], v[176:191]\n"
"v_mfma_f32_32x32x16_bf16 v[160:175], a[104:107], v[96:99], v[160:175]\n"
"s_waitcnt vmcnt(2)\n"
"v_mfma_f32_32x32x16_bf16 v[176:191], a[164:167], v[100:103], v[176:191]\n"
"v_mfma_f32_32x32x16_bf16 v[160:175], a[160:163], v[100:103], v[160:175]\n"
"s_waitcnt vmcnt(1)\n"
"v_mfma_f32_32x32x16_bf16 v[176:191], a[220:223], v[104:107], v[176:191]\n"
"s_nop 6\n"
"v_mfma_f32_32x32x16_bf16 v[160:175], a[216:219], v[104:107], v[160:175]\n"
"s_nop 3\n"
"ds_write_b128 v127, v[176:179] offset:36864\n"
"ds_write_b128 v127, v[180:183] offset:37888\n"
"ds_write_b128 v12, v[184:187] offset:38912\n"
"ds_write_b128 v12, v[188:191] offset:39936\n"
"s_nop 3\n"
"ds_write_b128 v126, v[168:171] offset:34816\n"
"ds_write_b128 v126, v[172:175] offset:35840\n"
"v_pk_fma_f32 v[160:161], v[160:161], v[42:43], v[28:29] op_sel_hi:[1,0,1]\n"
"v_pk_fma_f32 v[162:163], v[162:163], v[42:43], v[30:31] op_sel_hi:[1,0,1]\n"
"v_pk_fma_f32 v[164:165], v[164:165], v[42:43], v[32:33] op_sel_hi:[1,0,1]\n"
"v_pk_fma_f32 v[166:167], v[166:167], v[42:43], v[34:35] op_sel_hi:[1,0,1]\n"
"s_waitcnt lgkmcnt(0)\n"
"s_barrier\n"
"ds_read_b128 v[224:227], v125 offset:34816\n"
"ds_read_b128 v[232:235], v125 offset:36864\n"
"ds_read_b128 v[240:243], v125 offset:38912\n"
"ds_read_b128 v[228:231], v125 offset:35840\n"
"ds_read_b128 v[236:239], v125 offset:37888\n"
"ds_read_b128 v[244:247], v125 offset:39936\n"
"s_waitcnt lgkmcnt(5)\n"
"v_pk_fma_f32 v[160:161], v[224:225], v[42:43], v[160:161] op_sel_hi:[1,0,1]\n"
"v_pk_fma_f32 v[162:163], v[226:227], v[42:43], v[162:163] op_sel_hi:[1,0,1]\n"
"s_waitcnt lgkmcnt(4)\n"
"v_pk_fma_f32 v[160:161], v[232:233], v[42:43], v[160:161] op_sel_hi:[1,0,1]\n"
"v_pk_fma_f32 v[162:163], v[234:235], v[42:43], v[162:163] op_sel_hi:[1,0,1]\n"
"s_waitcnt lgkmcnt(3)\n"
"v_pk_fma_f32 v[160:161], v[240:241], v[42:43], v[160:161] op_sel_hi:[1,0,1]\n"
"v_pk_fma_f32 v[162:163], v[242:243], v[42:43], v[162:163] op_sel_hi:[1,0,1]\n"
"v_cvt_pk_bf16_f32 v160, v160, v161\n"
"v_cvt_pk_bf16_f32 v161, v162, v163\n"
"s_waitcnt lgkmcnt(2)\n"
"v_pk_fma_f32 v[164:165], v[228:229], v[42:43], v[164:165] op_sel_hi:[1,0,1]\n"
"v_pk_fma_f32 v[166:167], v[230:231], v[42:43], v[166:167] op_sel_hi:[1,0,1]\n"
"s_waitcnt lgkmcnt(1)\n"
"v_pk_fma_f32 v[164:165], v[236:237], v[42:43], v[164:165] op_sel_hi:[1,0,1]\n"
"v_pk_fma_f32 v[166:167], v[238:239], v[42:43], v[166:167] op_sel_hi:[1,0,1]\n"
"s_waitcnt lgkmcnt(0)\n"
"v_pk_fma_f32 v[164:165], v[244:245], v[42:43], v[164:165] op_sel_hi:[1,0,1]\n"
"v_pk_fma_f32 v[166:167], v[246:247], v[42:43], v[166:167] op_sel_hi:[1,0,1]\n"
"v_cvt_pk_bf16_f32 v162, v164, v165\n"
"v_cvt_pk_bf16_f32 v163, v166, v167\n"
"buffer_store_dwordx4 v[160:163], v124, s[48:51], s35 offen\n"
"L_end:\n"
"s_endpgm\n"
"s_waitcnt vmcnt(0)\n"
"s_waitcnt lgkmcnt(0)\n"
"L_kslow:\n"
"s_mov_b32 s62, 0x400000\n"
"L_kspin:\n"
"buffer_load_dwordx2 v[8:9], v44, s[40:43], 0 offen offset:0 sc1\n"
"s_waitcnt vmcnt(0)\n"
"s_sub_u32 s62, s62, 1\n"
"s_cbranch_scc1 L_kback\n"
"v_cmp_ne_u64 vcc, s[58:59], v[8:9]\n"
"s_cbranch_vccnz L_kspin\n"
"s_branch L_kback\n"
"L_tslow:\n"
"s_mov_b32 s62, 0x400000\n"
"L_tspin:\n"
"buffer_load_dwordx4 v[28:31], v17, s[40:43], 0 offen offset:128 sc1\n"
"s_waitcnt vmcnt(0)\n"
"s_sub_u32 s62, s62, 1\n"
"s_cbranch_scc1 L_tback\n"
"v_cmp_ne_u64 vcc, s[58:59], v[28:29]\n"
"s_cbranch_vccnz L_tspin\n"
"v_cmp_ne_u64 vcc, s[58:59], v[30:31]\n"
"s_cbranch_vccnz L_tspin\n"
"s_branch L_tback\n"
"L_rslow:\n"
"s_mov_b32 s62, 0x400000\n"
"L_rspin:\n"
"buffer_load_dwordx2 v[32:33], v255, s[40:43], 0 offen offset:136 sc1\n"
"s_waitcnt vmcnt(0)\n"
"s_sub_u32 s62, s62, 1\n"
"s_cbranch_scc1 L_rback\n"
"v_cmp_ne_u64 vcc, s[58:59], v[32:33]\n"
"s_cbranch_vccnz L_rspin\n"
"s_branch L_rback\n"
"L_padp:\n"
"s_cbranch_execz L_ret10\n"
"L_p0slow:\n"
"s_mov_b32 s57, 0x400000\n"
"v_mov_b32 v19, 0\n"
"buffer_atomic_add v19, v5, s[40:43], s61 offen offset:256 sc0\n"
"s_waitcnt vmcnt(1)\n"
"v_cmp_eq_u32 vcc, 16, v18\n"
"s_cbranch_vccnz L_p0back\n"
"v_mov_b32 v6, 0\n"
"buffer_atomic_add v6, v5, s[40:43], s61 offen offset:256 sc0\n"
"s_sleep 2\n"
"L_p0spin:\n"
"v_mov_b32 v18, 0\n"
"buffer_atomic_add v18, v5, s[40:43], s61 offen offset:256 sc0\n"
"s_waitcnt vmcnt(2)\n"
"v_cmp_eq_u32 vcc, 16, v19\n"
"s_cbranch_vccnz L_p0back\n"
"v_mov_b32 v19, 0\n"
"buffer_atomic_add v19, v5, s[40:43], s61 offen offset:256 sc0\n"
"s_waitcnt vmcnt(2)\n"
"v_cmp_eq_u32 vcc, 16, v6\n"
"s_cbranch_vccnz L_p0back\n"
"v_mov_b32 v6, 0\n"
"buffer_atomic_add v6, v5, s[40:43], s61 offen offset:256 sc0\n"
"s_waitcnt vmcnt(2)\n"
"v_cmp_eq_u32 vcc, 16, v18\n"
"s_cbranch_vccnz L_p0back\n"
"s_sub_u32 s57, s57, 1\n"
"s_cbranch_scc0 L_p0spin\n"
"s_branch L_p0back\n"
"L_p1slow:\n"
"s_mov_b32 s57, 0x400000\n"
"v_mov_b32 v17, 0\n"
"buffer_atomic_add v17, v4, s[40:43], s61 offen offset:384 sc0\n"
"s_waitcnt vmcnt(2)\n"
"v_cmp_eq_u32 vcc, 16, v9\n"
"s_cbranch_vccnz L_p1back\n"
"v_mov_b32 v8, 0\n"
"buffer_atomic_add v8, v4, s[40:43], s61 offen offset:384 sc0\n"
"s_sleep 2\n"
"L_p1spin:\n"
"v_mov_b32 v9, 0\n"
"buffer_atomic_add v9, v4, s[40:43], s61 offen offset:384 sc0\n"
"s_waitcnt vmcnt(2)\n"
"v_cmp_eq_u32 vcc, 16, v17\n"
"s_cbranch_vccnz L_p1back\n"
"v_mov_b32 v17, 0\n"
"buffer_atomic_add v17, v4, s[40:43], s61 offen offset:384 sc0\n"
"s_waitcnt vmcnt(2)\n"
"v_cmp_eq_u32 vcc, 16, v8\n"
"s_cbranch_vccnz L_p1back\n"
"v_mov_b32 v8, 0\n"
"buffer_atomic_add v8, v4, s[40:43], s61 offen offset:384 sc0\n"
"s_waitcnt vmcnt(2)\n"
"v_cmp_eq_u32 vcc, 16, v9\n"
"s_cbranch_vccnz L_p1back\n"
"s_sub_u32 s57, s57, 1\n"
"s_cbranch_scc0 L_p1spin\n"
"s_branch L_p1back\n"
"L_p2slow:\n"
"s_mov_b32 s57, 0x400000\n"
"v_mov_b32 v19, 0\n"
"buffer_atomic_add v19, v5, s[40:43], s61 offen offset:260 sc0\n"
"s_waitcnt vmcnt(1)\n"
"v_cmp_eq_u32 vcc, 16, v18\n"
"s_cbranch_vccnz L_p2back\n"
"v_mov_b32 v6, 0\n"
"buffer_atomic_add v6, v5, s[40:43], s61 offen offset:260 sc0\n"
"s_sleep 2\n"
"L_p2spin:\n"
"v_mov_b32 v18, 0\n"
"buffer_atomic_add v18, v5, s[40:43], s61 offen offset:260 sc0\n"
"s_waitcnt vmcnt(2)\n"
"v_cmp_eq_u32 vcc, 16, v19\n"
"s_cbranch_vccnz L_p2back\n"
"v_mov_b32 v19, 0\n"
"buffer_atomic_add v19, v5, s[40:43], s61 offen offset:260 sc0\n"
"s_waitcnt vmcnt(2)\n"
"v_cmp_eq_u32 vcc, 16, v6\n"
"s_cbranch_vccnz L_p2back\n"
"v_mov_b32 v6, 0\n"
"buffer_atomic_add v6, v5, s[40:43], s61 offen offset:260 sc0\n"
"s_waitcnt vmcnt(2)\n"
"v_cmp_eq_u32 vcc, 16, v18\n"
"s_cbranch_vccnz L_p2back\n"
"s_sub_u32 s57, s57, 1\n"
"s_cbranch_scc0 L_p2spin\n"
"s_branch L_p2back\n"
"L_p3slow:\n"
"s_mov_b32 s57, 0x400000\n"
"v_mov_b32 v17, 0\n"
"buffer_atomic_add v17, v5, s[40:43], s61 offen offset:388 sc0\n"
"s_waitcnt vmcnt(2)\n"
"v_cmp_eq_u32 vcc, 16, v9\n"
"s_cbranch_vccnz L_p3back\n"
"v_mov_b32 v8, 0\n"
"buffer_atomic_add v8, v5, s[40:43], s61 offen offset:388 sc0\n"
"s_sleep 2\n"
"L_p3spin:\n"
"v_mov_b32 v9, 0\n"
"buffer_atomic_add v9, v5, s[40:43], s61 offen offset:388 sc0\n"
"s_waitcnt vmcnt(2)\n"
"v_cmp_eq_u32 vcc, 16, v17\n"
"s_cbranch_vccnz L_p3back\n"
"v_mov_b32 v17, 0\n"
"buffer_atomic_add v17, v5, s[40:43], s61 offen offset:388 sc0\n"
"s_waitcnt vmcnt(2)\n"
"v_cmp_eq_u32 vcc, 16, v8\n"
"s_cbranch_vccnz L_p3back\n"
"v_mov_b32 v8, 0\n"
"buffer_atomic_add v8, v5, s[40:43], s61 offen offset:388 sc0\n"
"s_waitcnt vmcnt(2)\n"
"v_cmp_eq_u32 vcc, 16, v9\n"
"s_cbranch_vccnz L_p3back\n"
"s_sub_u32 s57, s57, 1\n"
"s_cbranch_scc0 L_p3spin\n"
"s_branch L_p3back\n"
"L_p4slow:\n"
"s_mov_b32 s57, 0x400000\n"
"v_mov_b32 v19, 0\n"
"buffer_atomic_add v19, v5, s[40:43], s61 offen offset:264 sc0\n"
"s_waitcnt vmcnt(4)\n"
"v_cmp_eq_u32 vcc, 16, v18\n"
"s_cbranch_vccnz L_p4back\n"
"v_mov_b32 v6, 0\n"
"buffer_atomic_add v6, v5, s[40:43], s61 offen offset:264 sc0\n"
"s_sleep 2\n"
"L_p4spin:\n"
"v_mov_b32 v18, 0\n"
"buffer_atomic_add v18, v5, s[40:43], s61 offen offset:264 sc0\n"
"s_waitcnt vmcnt(2)\n"
"v_cmp_eq_u32 vcc, 16, v19\n"
"s_cbranch_vccnz L_p4back\n"
"v_mov_b32 v19, 0\n"
"buffer_atomic_add v19, v5, s[40:43], s61 offen offset:264 sc0\n"
"s_waitcnt vmcnt(2)\n"
"v_cmp_eq_u32 vcc, 16, v6\n"
"s_cbranch_vccnz L_p4back\n"
"v_mov_b32 v6, 0\n"
"buffer_atomic_add v6, v5, s[40:43], s61 offen offset:264 sc0\n"
"s_waitcnt vmcnt(2)\n"
"v_cmp_eq_u32 vcc, 16, v18\n"
"s_cbranch_vccnz L_p4back\n"
"s_sub_u32 s57, s57, 1\n"
"s_cbranch_scc0 L_p4spin\n"
"s_branch L_p4back\n"
"L_p5slow:\n"
"s_mov_b32 s57, 0x400000\n"
"v_mov_b32 v17, 0\n"
"buffer_atomic_add v17, v4, s[40:43], s61 offen offset:392 sc0\n"
"s_waitcnt vmcnt(8)\n"
"v_cmp_eq_u32 vcc, 16, v9\n"
"s_cbranch_vccnz L_p5back\n"
"v_mov_b32 v8, 0\n"
"buffer_atomic_add v8, v4, s[40:43], s61 offen offset:392 sc0\n"
"s_sleep 2\n"
"L_p5spin:\n"
"v_mov_b32 v9, 0\n"
"buffer_atomic_add v9, v4, s[40:43], s61 offen offset:392 sc0\n"
"s_waitcnt vmcnt(2)\n"
"v_cmp_eq_u32 vcc, 16, v17\n"
"s_cbranch_vccnz L_p5back\n"
"v_mov_b32 v17, 0\n"
"buffer_atomic_add v17, v4, s[40:43], s61 offen offset:392 sc0\n"
"s_waitcnt vmcnt(2)\n"
"v_cmp_eq_u32 vcc, 16, v8\n"
"s_cbranch_vccnz L_p5back\n"
"v_mov_b32 v8, 0\n"
"buffer_atomic_add v8, v4, s[40:43], s61 offen offset:392 sc0\n"
"s_waitcnt vmcnt(2)\n"
"v_cmp_eq_u32 vcc, 16, v9\n"
"s_cbranch_vccnz L_p5back\n"
"s_sub_u32 s57, s57, 1\n"
"s_cbranch_scc0 L_p5spin\n"
"s_branch L_p5back\n"
"L_p6slow:\n"
"s_mov_b32 s57, 0x400000\n"
"v_mov_b32 v19, 0\n"
"buffer_atomic_add v19, v5, s[40:43], s61 offen offset:268 sc0\n"
"s_waitcnt vmcnt(5)\n"
"v_cmp_eq_u32 vcc, 16, v18\n"
"s_cbranch_vccnz L_p6back\n"
"v_mov_b32 v6, 0\n"
"buffer_atomic_add v6, v5, s[40:43], s61 offen offset:268 sc0\n"
"s_sleep 2\n"
"L_p6spin:\n"
"v_mov_b32 v18, 0\n"
"buffer_atomic_add v18, v5, s[40:43], s61 offen offset:268 sc0\n"
"s_waitcnt vmcnt(2)\n"
"v_cmp_eq_u32 vcc, 16, v19\n"
"s_cbranch_vccnz L_p6back\n"
"v_mov_b32 v19, 0\n"
"buffer_atomic_add v19, v5, s[40:43], s61 offen offset:268 sc0\n"
"s_waitcnt vmcnt(2)\n"
"v_cmp_eq_u32 vcc, 16, v6\n"
"s_cbranch_vccnz L_p6back\n"
"v_mov_b32 v6, 0\n"
"buffer_atomic_add v6, v5, s[40:43], s61 offen offset:268 sc0\n"
"s_waitcnt vmcnt(2)\n"
"v_cmp_eq_u32 vcc, 16, v18\n"
"s_cbranch_vccnz L_p6back\n"
"s_sub_u32 s57, s57, 1\n"
"s_cbranch_scc0 L_p6spin\n"
"s_branch L_p6back\n"
"L_p7slow:\n"
"s_mov_b32 s57, 0x400000\n"
"v_mov_b32 v17, 0\n"
"buffer_atomic_add v17, v5, s[40:43], s61 offen offset:396 sc0\n"
"s_waitcnt vmcnt(11)\n"
"v_cmp_eq_u32 vcc, 16, v9\n"
"s_cbranch_vccnz L_p7back\n"
"v_mov_b32 v8, 0\n"
"buffer_atomic_add v8, v5, s[40:43], s61 offen offset:396 sc0\n"
"s_sleep 2\n"
"L_p7spin:\n"
"v_mov_b32 v9, 0\n"
"buffer_atomic_add v9, v5, s[40:43], s61 offen offset:396 sc0\n"
"s_waitcnt vmcnt(2)\n"
"v_cmp_eq_u32 vcc, 16, v17\n"
"s_cbranch_vccnz L_p7back\n"
"v_mov_b32 v17, 0\n"
"buffer_atomic_add v17, v5, s[40:43], s61 offen offset:396 sc0\n"
"s_waitcnt vmcnt(2)\n"
"v_cmp_eq_u32 vcc, 16, v8\n"
"s_cbranch_vccnz L_p7back\n"
"v_mov_b32 v8, 0\n"
"buffer_atomic_add v8, v5, s[40:43], s61 offen offset:396 sc0\n"
"s_waitcnt vmcnt(2)\n"
"v_cmp_eq_u32 vcc, 16, v9\n"
"s_cbranch_vccnz L_p7back\n"
"s_sub_u32 s57, s57, 1\n"
"s_cbranch_scc0 L_p7spin\n"
"s_branch L_p7back\n"
"L_ep:\n"
"s_mov_b64 exec, 1\n"
"s_mov_b32 s60, 131072\n"
"buffer_store_dwordx2 v[18:19], off, s[40:43], s60 sc1\n"
"s_mov_b64 exec, -1\n"
"s_branch L_epb\n"
"L_fallback:\n"
"s_waitcnt vmcnt(0)\n"
"s_waitcnt lgkmcnt(0)\n"
"s_mov_b32 s68, -1\n"
"s_mov_b32 s64, s96\n"
"s_movk_i32 s65, 0x800\n"
"s_mov_b32 s62, 32\n"
"v_mov_b32 v8, 0xff800000\n"
"v_mov_b32 v9, 0xff800000\n"
"v_mov_b32 v10, 0xff800000\n"
"v_mov_b32 v11, 0xff800000\n"
"L_fb1:\n"
"buffer_load_dwordx4 a[244:247], v2, s[40:43], s64 offen sc1\n"
"s_waitcnt vmcnt(0)\n"
"v_mfma_f32_32x32x16_bf16 v[208:223], a[244:247], v[128:131], 0\n"
"s_nop 11\n"
"v_max3_f32 v8, v208, v209, v8\n"
"v_max3_f32 v8, v210, v211, v8\n"
"v_max3_f32 v8, v212, v213, v8\n"
"v_max3_f32 v8, v214, v215, v8\n"
"v_max3_f32 v8, v216, v217, v8\n"
"v_max3_f32 v8, v218, v219, v8\n"
"v_max3_f32 v8, v220, v221, v8\n"
"v_max3_f32 v8, v222, v223, v8\n"
"v_mfma_f32_32x32x16_bf16 v[208:223], a[244:247], v[132:135], 0\n"
"s_nop 11\n"
"v_max3_f32 v9, v208, v209, v9\n"
"v_max3_f32 v9, v210, v211, v9\n"
"v_max3_f32 v9, v212, v213, v9\n"
"v_max3_f32 v9, v214, v215, v9\n"
"v_max3_f32 v9, v216, v217, v9\n"
"v_max3_f32 v9, v218, v219, v9\n"
"v_max3_f32 v9, v220, v221, v9\n"
"v_max3_f32 v9, v222, v223, v9\n"
"v_mfma_f32_32x32x16_bf16 v[208:223], a[244:247], v[136:139], 0\n"
"s_nop 11\n"
"v_max3_f32 v10, v208, v209, v10\n"
"v_max3_f32 v10, v210, v211, v10\n"
"v_max3_f32 v10, v212, v213, v10\n"
"v_max3_f32 v10, v214, v215, v10\n"
"v_max3_f32 v10, v216, v217, v10\n"
"v_max3_f32 v10, v218, v219, v10\n"
"v_max3_f32 v10, v220, v221, v10\n"
"v_max3_f32 v10, v222, v223, v10\n"
"v_mfma_f32_32x32x16_bf16 v[208:223], a[244:247], v[140:143], 0\n"
"s_nop 11\n"
"v_max3_f32 v11, v208, v209, v11\n"
"v_max3_f32 v11, v210, v211, v11\n"
"v_max3_f32 v11, v212, v213, v11\n"
"v_max3_f32 v11, v214, v215, v11\n"
"v_max3_f32 v11, v216, v217, v11\n"
"v_max3_f32 v11, v218, v219, v11\n"
"v_max3_f32 v11, v220, v221, v11\n"
"v_max3_f32 v11, v222, v223, v11\n"
"s_add_u32 s64, s64, s65\n"
"s_xor_b32 s65, s65, 0x1000\n"
"s_sub_u32 s62, s62, 1\n"
"s_cmp_lg_u32 s62, 0\n"
"s_cbranch_scc1 L_fb1\n"
"v_mov_b32 v12, v8\n"
"v_mov_b32 v13, v9\n"
"v_mov_b32 v14, v10\n"
"v_mov_b32 v15, v11\n"
"v_permlane32_swap_b32 v8, v12\n"
"v_permlane32_swap_b32 v9, v13\n"
"v_permlane32_swap_b32 v10, v14\n"
"v_permlane32_swap_b32 v11, v15\n"
"v_max_f32 v8, v8, v12\n"
"v_max_f32 v9, v9, v13\n"
"v_max_f32 v10, v10, v14\n"
"v_max_f32 v11, v11, v15\n"
"v_xor_b32 v16, 0x400, v251\n"
"s_barrier\n"
"ds_write_b128 v251, v[8:11]\n"
"s_waitcnt lgkmcnt(0)\n"
"s_barrier\n"
"ds_read_b128 v[12:15], v16\n"
"v_mov_b64 v[144:145], 0\n"
"v_mov_b64 v[146:147], 0\n"
"v_mov_b64 v[148:149], 0\n"
"v_mov_b64 v[150:151], 0\n"
"v_mov_b64 v[152:153], 0\n"
"v_mov_b64 v[154:155], 0\n"
"v_mov_b64 v[156:157], 0\n"
"v_mov_b64 v[158:159], 0\n"
"v_mov_b64 v[160:161], 0\n"
"v_mov_b64 v[162:163], 0\n"
"v_mov_b64 v[164:165], 0\n"
"v_mov_b64 v[166:167], 0\n"
"v_mov_b64 v[168:169], 0\n"
"v_mov_b64 v[170:171], 0\n"
"v_mov_b64 v[172:173], 0\n"
"v_mov_b64 v[174:175], 0\n"
"v_mov_b64 v[176:177], 0\n"
"v_mov_b64 v[178:179], 0\n"
"v_mov_b64 v[180:181], 0\n"
"v_mov_b64 v[182:183], 0\n"
"v_mov_b64 v[184:185], 0\n"
"v_mov_b64 v[186:187], 0\n"
"v_mov_b64 v[188:189], 0\n"
"v_mov_b64 v[190:191], 0\n"
"v_mov_b64 v[192:193], 0\n"
"v_mov_b64 v[194:195], 0\n"
"v_mov_b64 v[196:197], 0\n"
"v_mov_b64 v[198:199], 0\n"
"v_mov_b64 v[200:201], 0\n"
"v_mov_b64 v[202:203], 0\n"
"v_mov_b64 v[204:205], 0\n"
"v_mov_b64 v[206:207], 0\n"
"s_waitcnt lgkmcnt(0)\n"
"v_max_f32 v8, v8, v14\n"
"v_max_f32 v9, v9, v15\n"
"v_max_f32 v10, v10, v12\n"
"v_max_f32 v11, v11, v13\n"
"s_barrier\n"
"s_mov_b32 s62, 32\n"
"s_mov_b32 s64, s96\n"
"L_fb2:\n"
"s_mov_b64 exec, s[76:77]\n"
"buffer_load_dwordx4 a[248:251], v47, s[40:43], s64 offen offset:1024 sc1\n"
"buffer_load_dwordx4 a[252:255], v47, s[40:43], s64 offen offset:1536 sc1\n"
"s_mov_b64 exec, -1\n"
"buffer_load_dwordx4 a[244:247], v2, s[40:43], s64 offen sc1\n"
"s_waitcnt vmcnt(0)\n"
"v_mfma_f32_32x32x16_bf16 v[208:223], a[244:247], v[128:131], 0\n"
"s_nop 11\n"
"v_sub_f32 v208, v208, v8\n"
"v_exp_f32 v208, v208\n"
"v_sub_f32 v209, v209, v8\n"
"v_exp_f32 v209, v209\n"
"v_sub_f32 v210, v210, v8\n"
"v_exp_f32 v210, v210\n"
"v_sub_f32 v211, v211, v8\n"
"v_exp_f32 v211, v211\n"
"v_sub_f32 v212, v212, v8\n"
"v_exp_f32 v212, v212\n"
"v_sub_f32 v213, v213, v8\n"
"v_exp_f32 v213, v213\n"
"v_sub_f32 v214, v214, v8\n"
"v_exp_f32 v214, v214\n"
"v_sub_f32 v215, v215, v8\n"
"v_exp_f32 v215, v215\n"
"v_sub_f32 v216, v216, v8\n"
"v_exp_f32 v216, v216\n"
"v_sub_f32 v217, v217, v8\n"
"v_exp_f32 v217, v217\n"
"v_sub_f32 v218, v218, v8\n"
"v_exp_f32 v218, v218\n"
"v_sub_f32 v219, v219, v8\n"
"v_exp_f32 v219, v219\n"
"v_sub_f32 v220, v220, v8\n"
"v_exp_f32 v220, v220\n"
"v_sub_f32 v221, v221, v8\n"
"v_exp_f32 v221, v221\n"
"v_sub_f32 v222, v222, v8\n"
"v_exp_f32 v222, v222\n"
"v_sub_f32 v223, v223, v8\n"
"v_exp_f32 v223, v223\n"
"v_smfmac_f32_32x32x32_bf16 v[144:159], a[248:251], v[208:215], v45\n"
"s_nop 0\n"
"v_smfmac_f32_32x32x32_bf16 v[144:159], a[252:255], v[216:223], v45\n"
"v_mfma_f32_32x32x16_bf16 v[208:223], a[244:247], v[132:135], 0\n"
"s_nop 11\n"
"v_sub_f32 v208, v208, v9\n"
"v_exp_f32 v208, v208\n"
"v_sub_f32 v209, v209, v9\n"
"v_exp_f32 v209, v209\n"
"v_sub_f32 v210, v210, v9\n"
"v_exp_f32 v210, v210\n"
"v_sub_f32 v211, v211, v9\n"
"v_exp_f32 v211, v211\n"
"v_sub_f32 v212, v212, v9\n"
"v_exp_f32 v212, v212\n"
"v_sub_f32 v213, v213, v9\n"
"v_exp_f32 v213, v213\n"
"v_sub_f32 v214, v214, v9\n"
"v_exp_f32 v214, v214\n"
"v_sub_f32 v215, v215, v9\n"
"v_exp_f32 v215, v215\n"
"v_sub_f32 v216, v216, v9\n"
"v_exp_f32 v216, v216\n"
"v_sub_f32 v217, v217, v9\n"
"v_exp_f32 v217, v217\n"
"v_sub_f32 v218, v218, v9\n"
"v_exp_f32 v218, v218\n"
"v_sub_f32 v219, v219, v9\n"
"v_exp_f32 v219, v219\n"
"v_sub_f32 v220, v220, v9\n"
"v_exp_f32 v220, v220\n"
"v_sub_f32 v221, v221, v9\n"
"v_exp_f32 v221, v221\n"
"v_sub_f32 v222, v222, v9\n"
"v_exp_f32 v222, v222\n"
"v_sub_f32 v223, v223, v9\n"
"v_exp_f32 v223, v223\n"
"v_smfmac_f32_32x32x32_bf16 v[160:175], a[248:251], v[208:215], v45\n"
"s_nop 0\n"
"v_smfmac_f32_32x32x32_bf16 v[160:175], a[252:255], v[216:223], v45\n"
"v_mfma_f32_32x32x16_bf16 v[208:223], a[244:247], v[136:139], 0\n"
"s_nop 11\n"
"v_sub_f32 v208, v208, v10\n"
"v_exp_f32 v208, v208\n"
"v_sub_f32 v209, v209, v10\n"
"v_exp_f32 v209, v209\n"
"v_sub_f32 v210, v210, v10\n"
"v_exp_f32 v210, v210\n"
"v_sub_f32 v211, v211, v10\n"
"v_exp_f32 v211, v211\n"
"v_sub_f32 v212, v212, v10\n"
"v_exp_f32 v212, v212\n"
"v_sub_f32 v213, v213, v10\n"
"v_exp_f32 v213, v213\n"
"v_sub_f32 v214, v214, v10\n"
"v_exp_f32 v214, v214\n"
"v_sub_f32 v215, v215, v10\n"
"v_exp_f32 v215, v215\n"
"v_sub_f32 v216, v216, v10\n"
"v_exp_f32 v216, v216\n"
"v_sub_f32 v217, v217, v10\n"
"v_exp_f32 v217, v217\n"
"v_sub_f32 v218, v218, v10\n"
"v_exp_f32 v218, v218\n"
"v_sub_f32 v219, v219, v10\n"
"v_exp_f32 v219, v219\n"
"v_sub_f32 v220, v220, v10\n"
"v_exp_f32 v220, v220\n"
"v_sub_f32 v221, v221, v10\n"
"v_exp_f32 v221, v221\n"
"v_sub_f32 v222, v222, v10\n"
"v_exp_f32 v222, v222\n"
"v_sub_f32 v223, v223, v10\n"
"v_exp_f32 v223, v223\n"
"v_smfmac_f32_32x32x32_bf16 v[176:191], a[248:251], v[208:215], v45\n"
"s_nop 0\n"
"v_smfmac_f32_32x32x32_bf16 v[176:191], a[252:255], v[216:223], v45\n"
"v_mfma_f32_32x32x16_bf16 v[208:223], a[244:247], v[140:143], 0\n"
"s_nop 11\n"
"v_sub_f32 v208, v208, v11\n"
"v_exp_f32 v208, v208\n"
"v_sub_f32 v209, v209, v11\n"
"v_exp_f32 v209, v209\n"
"v_sub_f32 v210, v210, v11\n"
"v_exp_f32 v210, v210\n"
"v_sub_f32 v211, v211, v11\n"
"v_exp_f32 v211, v211\n"
"v_sub_f32 v212, v212, v11\n"
"v_exp_f32 v212, v212\n"
"v_sub_f32 v213, v213, v11\n"
"v_exp_f32 v213, v213\n"
"v_sub_f32 v214, v214, v11\n"
"v_exp_f32 v214, v214\n"
"v_sub_f32 v215, v215, v11\n"
"v_exp_f32 v215, v215\n"
"v_sub_f32 v216, v216, v11\n"
"v_exp_f32 v216, v216\n"
"v_sub_f32 v217, v217, v11\n"
"v_exp_f32 v217, v217\n"
"v_sub_f32 v218, v218, v11\n"
"v_exp_f32 v218, v218\n"
"v_sub_f32 v219, v219, v11\n"
"v_exp_f32 v219, v219\n"
"v_sub_f32 v220, v220, v11\n"
"v_exp_f32 v220, v220\n"
"v_sub_f32 v221, v221, v11\n"
"v_exp_f32 v221, v221\n"
"v_sub_f32 v222, v222, v11\n"
"v_exp_f32 v222, v222\n"
"v_sub_f32 v223, v223, v11\n"
"v_exp_f32 v223, v223\n"
"v_smfmac_f32_32x32x32_bf16 v[192:207], a[248:251], v[208:215], v45\n"
"s_nop 0\n"
"v_smfmac_f32_32x32x32_bf16 v[192:207], a[252:255], v[216:223], v45\n"
"s_add_u32 s64, s64, s65\n"
"s_xor_b32 s65, s65, 0x1000\n"
"s_sub_u32 s62, s62, 1\n"
"s_cmp_lg_u32 s62, 0\n"
"s_nop 15\n"
"s_cbranch_scc1 L_fb2\n"
"ds_write_b128 v41, v[192:195] offset:32768\n"
"ds_write_b128 v41, v[196:199] offset:33792\n"
"ds_write_b32 v42, v200 offset:34816\n"
"s_cmp_eq_u32 s69, 0\n"
"s_mov_b32 s69, 0\n"
"s_cbranch_scc1 L_Bepi\n"
"ds_write_b128 v41, v[176:179] offset:0\n"
"ds_write_b128 v41, v[180:183] offset:1024\n"
"ds_write_b32 v42, v184 offset:2048\n"
"s_waitcnt lgkmcnt(0)\n"
"s_barrier\n"
"ds_read_b32 v28, v126 offset:2048\n"
"ds_read_b128 v[20:23], v43 offset:0\n"
"ds_read_b128 v[24:27], v43 offset:1024\n"
"s_waitcnt lgkmcnt(0)\n"
"v_add_f32 v152, v152, v28\n"
"v_rcp_f32 v152, v152\n"
"v_pk_add_f32 v[144:145], v[144:145], v[20:21]\n"
"v_pk_add_f32 v[146:147], v[146:147], v[22:23]\n"
"v_pk_add_f32 v[148:149], v[148:149], v[24:25]\n"
"v_pk_add_f32 v[150:151], v[150:151], v[26:27]\n"
"v_pk_mul_f32 v[144:145], v[144:145], v[152:153] op_sel_hi:[1,0]\n"
"v_pk_mul_f32 v[146:147], v[146:147], v[152:153] op_sel_hi:[1,0]\n"
"v_pk_mul_f32 v[148:149], v[148:149], v[152:153] op_sel_hi:[1,0]\n"
"v_pk_mul_f32 v[150:151], v[150:151], v[152:153] op_sel_hi:[1,0]\n"
"v_cvt_pk_bf16_f32 v20, v144, v145\n"
"v_cvt_pk_bf16_f32 v21, v146, v147\n"
"v_cvt_pk_bf16_f32 v22, v148, v149\n"
"v_cvt_pk_bf16_f32 v23, v150, v151\n"
"buffer_store_dwordx4 v[20:23], v2, s[40:43], s32 offen sc1\n"
"s_waitcnt vmcnt(0)\n"
"s_barrier\n"
"buffer_atomic_add v7, v3, s[40:43], 0 offen offset:256\n"
"s_waitcnt vmcnt(0)\n"
"s_branch L_Bepi\n"
"L_last:\n"
".pushsection .rodata\n"
".fill 640, 4, 0\n"
".popsection\n"
  :: "s"(__builtin_amdgcn_kernarg_segment_ptr()), "s"(blockIdx.x), "v"(threadIdx.x), "s"((unsigned)(size_t)lds)
  : "memory", "vcc", "scc", "s8", "s9", "s10", "s11", "s12", "s13", "s14", "s15", "s16", "s17", "s18", "s19", "s20", "s21", "s22", "s23", "s24", "s25", "s26", "s27", "s28", "s29", "s30", "s31", "s36", "s37", "s38", "s39", "s40", "s41", "s42", "s43", "s44", "s45", "s46", "s47", "s48", "s49", "s50", "s51", "s52", "s53", "s54", "s55", "s56", "s57", "s58", "s59", "s60", "s61", "s62", "s63", "s64", "s65", "s66", "s67", "s68", "s69", "s70", "s71", "s72", "s73", "s74", "s75", "s76", "s77", "s78", "s79", "s80", "s81", "s82", "s83", "s84", "s85", "s86", "s87", "s88", "s89", "s90", "s91", "s92", "s93", "s94", "s95", "s96", "s97", "s98", "s99", "v1", "v2", "v3", "v4", "v5", "v6", "v7", "v8", "v9", "v10", "v11", "v12", "v13", "v14", "v15", "v16", "v17", "v18", "v19", "v20", "v21", "v22", "v23", "v24", "v25", "v26", "v27", "v28", "v29", "v30", "v31", "v32", "v33", "v34", "v35", "v36", "v37", "v38", "v39", "v40", "v41", "v42", "v43", "v44", "v45", "v46", "v47", "v48", "v49", "v50", "v51", "v52", "v53", "v54", "v55", "v56", "v57", "v58", "v59", "v60", "v61", "v62", "v63", "v64", "v65", "v66", "v67", "v68", "v69", "v70", "v71", "v72", "v73", "v74", "v75", "v76", "v77", "v78", "v79", "v80", "v81", "v82", "v83", "v84", "v85", "v86", "v87", "v88", "v89", "v90", "v91", "v92", "v93", "v94", "v95", "v96", "v97", "v98", "v99", "v100", "v101", "v102", "v103", "v104", "v105", "v106", "v107", "v108", "v109", "v110", "v111", "v112", "v113", "v114", "v115", "v116", "v117", "v118", "v119", "v120", "v121", "v122", "v123", "v124", "v125", "v126", "v127", "v128", "v129", "v130", "v131", "v132", "v133", "v134", "v135", "v136", "v137", "v138", "v139", "v140", "v141", "v142", "v143", "v144", "v145", "v146", "v147", "v148", "v149", "v150", "v151", "v152", "v153", "v154", "v155", "v156", "v157", "v158", "v159", "v160", "v161", "v162", "v163", "v164", "v165", "v166", "v167", "v168", "v169", "v170", "v171", "v172", "v173", "v174", "v175", "v176", "v177", "v178", "v179", "v180", "v181", "v182", "v183", "v184", "v185", "v186", "v187", "v188", "v189", "v190", "v191", "v192", "v193", "v194", "v195", "v196", "v197", "v198", "v199", "v200", "v201", "v202", "v203", "v204", "v205", "v206", "v207", "v208", "v209", "v210", "v211", "v212", "v213", "v214", "v215", "v216", "v217", "v218", "v219", "v220", "v221", "v222", "v223", "v224", "v225", "v226", "v227", "v228", "v229", "v230", "v231", "v232", "v233", "v234", "v235", "v236", "v237", "v238", "v239", "v240", "v241", "v242", "v243", "v244", "v245", "v246", "v247", "v248", "v249", "v250", "v251", "v252", "v253", "v254", "v255", "a0", "a1", "a2", "a3", "a4", "a5", "a6", "a7", "a8", "a9", "a10", "a11", "a12", "a13", "a14", "a15", "a16", "a17", "a18", "a19", "a20", "a21", "a22", "a23", "a24", "a25", "a26", "a27", "a28", "a29", "a30", "a31", "a32", "a33", "a34", "a35", "a36", "a37", "a38", "a39", "a40", "a41", "a42", "a43", "a44", "a45", "a46", "a47", "a48", "a49", "a50", "a51", "a52", "a53", "a54", "a55", "a56", "a57", "a58", "a59", "a60", "a61", "a62", "a63", "a64", "a65", "a66", "a67", "a68", "a69", "a70", "a71", "a72", "a73", "a74", "a75", "a76", "a77", "a78", "a79", "a80", "a81", "a82", "a83", "a84", "a85", "a86", "a87", "a88", "a89", "a90", "a91", "a92", "a93", "a94", "a95", "a96", "a97", "a98", "a99", "a100", "a101", "a102", "a103", "a104", "a105", "a106", "a107", "a108", "a109", "a110", "a111", "a112", "a113", "a114", "a115", "a116", "a117", "a118", "a119", "a120", "a121", "a122", "a123", "a124", "a125", "a126", "a127", "a128", "a129", "a130", "a131", "a132", "a133", "a134", "a135", "a136", "a137", "a138", "a139", "a140", "a141", "a142", "a143", "a144", "a145", "a146", "a147", "a148", "a149", "a150", "a151", "a152", "a153", "a154", "a155", "a156", "a157", "a158", "a159", "a160", "a161", "a162", "a163", "a164", "a165", "a166", "a167", "a168", "a169", "a170", "a171", "a172", "a173", "a174", "a175", "a176", "a177", "a178", "a179", "a180", "a181", "a182", "a183", "a184", "a185", "a186", "a187", "a188", "a189", "a190", "a191", "a192", "a193", "a194", "a195", "a196", "a197", "a198", "a199", "a200", "a201", "a202", "a203", "a204", "a205", "a206", "a207", "a208", "a209", "a210", "a211", "a212", "a213", "a214", "a215", "a216", "a217", "a218", "a219", "a220", "a221", "a222", "a223", "a224", "a225", "a226", "a227", "a228", "a229", "a230", "a231", "a232", "a233", "a234", "a235", "a236", "a237", "a238", "a239", "a240", "a241", "a242", "a243", "a244", "a245", "a246", "a247", "a248", "a249", "a250", "a251", "a252", "a253", "a254", "a255");
}
extern "C" void kernel_launch(void* const* d_in, const int* in_sizes, int n_in, void* d_out, int out_size, void* d_ws,
                              size_t ws_size, hipStream_t stream) {
  blk_kernel<<<256, 256, 0, stream>>>(d_in[0], d_in[1], d_in[2], d_in[4], d_in[5], d_out, d_ws);
}
```
